```python
import torch

def _softmax(x, axis=-1):
    return torch.softmax(x, dim=axis)

def _gelu(x):
    return torch.nn.functional.gelu(x, approximate='tanh')

def _silu(x):
    return torch.nn.functional.silu(x)

def _sigmoid(x):
    return torch.sigmoid(x)

def _layernorm(x, axis=-1, eps=1e-05):
    m = x.mean(dim=axis, keepdim=True)
    v = x.var(dim=axis, keepdim=True, unbiased=False)
    return (x - m) / torch.sqrt(v + eps)

def _scatter_add(tgt, idx, src, axis=0):
    return tgt.clone().index_add_(axis, idx, src)

def _segment_sum(src, seg, n):
    out = torch.zeros((n,) + src.shape[1:], dtype=src.dtype)
    return out.index_add_(0, seg, src)

def _pad(x, pw):
    flat = tuple((v for pair in reversed(pw) for v in pair))
    return torch.nn.functional.pad(x, flat)
kernel_name = 'standalone__tree_speculative_verify_tree_attention_over_candidate_token_tree__sampling'

def setup_inputs():

    _w = lambda a: (lambda t: t.to(torch.bfloat16) if t.is_floating_point() else t)(torch.as_tensor(a))
    B, Hq, Hkv, T, C, D = (8, 32, 4, 32, 8192, 128)
    tree_mask = torch.zeros((B, T, T), dtype=float)
    for b in range(B):
        parent = torch.zeros(T, dtype=torch.int64)
        for i in range(1, T):
            parent[i] = int(torch.randint(0, i, (1,)).item())
        for i in range(T):
            j = i
            tree_mask[b, i, j] = 1.0
            while j > 0:
                j = parent[j]
                tree_mask[b, i, j] = 1.0
    return {'q': _w((lambda *s: torch.randn(*s, dtype=torch.bfloat16) * 0.8)(B, Hq, T, D)), 'k_cache': _w((lambda *s: torch.randn(*s, dtype=torch.bfloat16) * 0.8)(B, Hkv, C, D)), 'v_cache': _w((lambda *s: torch.randn(*s, dtype=torch.bfloat16) * 0.05)(B, Hkv, C, D)), 'k_tree': _w((lambda *s: torch.randn(*s, dtype=torch.bfloat16) * 0.8)(B, Hkv, T, D)), 'v_tree': _w((lambda *s: torch.randn(*s, dtype=torch.bfloat16) * 0.05)(B, Hkv, T, D)), 'tree_mask': _w(tree_mask)}

def reference(q, k_cache, v_cache, k_tree, v_tree, tree_mask):
    B, Hq, T, D = q.shape
    Hkv, C = (k_cache.shape[1], k_cache.shape[2])
    rep = Hq // Hkv
    kc = (lambda x, n, axis: torch.repeat_interleave(x, n, dim=axis))(k_cache, rep, axis=1)
    vc = (lambda x, n, axis: torch.repeat_interleave(x, n, dim=axis))(v_cache, rep, axis=1)
    kt = (lambda x, n, axis: torch.repeat_interleave(x, n, dim=axis))(k_tree, rep, axis=1)
    vt = (lambda x, n, axis: torch.repeat_interleave(x, n, dim=axis))(v_tree, rep, axis=1)
    k = torch.concatenate([kc, kt], axis=2)
    v = torch.concatenate([vc, vt], axis=2)
    scale = D ** (-0.5)
    scores = torch.einsum('bhqd,bhkd->bhqk', q, k) * scale
    ctx_mask = torch.ones((B, 1, T, C), dtype=scores.dtype)
    tm = tree_mask[:, None, :, :].clone()
    full_mask = torch.concatenate([ctx_mask, tm], axis=-1)
    scores = scores + (1.0 - full_mask) * -1000000000.0
    p = _softmax(scores, axis=-1)
    return torch.einsum('bhqk,bhkd->bhqd', p, v)
```

```cpp
#include <hip/hip_runtime.h>
#include <hip/hip_bf16.h>
#include <cstdio>
constexpr int B = 8, HQ = 32, HKV = 4, SQ = 32, SK = 8192, NSPLIT = 8;
constexpr float THR = 8.f;
constexpr int D = 128, NW = 8, QBLK = 32, KVBLK = 64;
constexpr float SCALE = 0.088388347648318447f;
constexpr int G = HQ / HKV, ROWS = G * SQ, NT = SK / KVBLK, TPS = NT / NSPLIT;
constexpr float L2E = 1.4426950408889634f, SL2E = SCALE * L2E, THR2 = THR * L2E;
constexpr size_t SHM_V = KVBLK * D * 2, SHM_K = KVBLK * D * 2, SHM_WS = 2 * SHM_V + 2 * SHM_K;
constexpr size_t SHM_TOT = SHM_WS + NW * 64 * 4;
constexpr size_t WS_PML = 0, PML_BYTES = (size_t)B * HQ * SQ * NSPLIT * 2 * 4;
constexpr size_t WS_PO = (WS_PML + PML_BYTES + 255) / 256 * 256;
using bf16 = __hip_bfloat16;
using bf16x8 = __attribute__((ext_vector_type(8))) short;
using s16x4 = __attribute__((ext_vector_type(4))) short;
using f32x16 = __attribute__((ext_vector_type(16))) float;
using f32x4 = __attribute__((ext_vector_type(4))) float;
using u32x4 = __attribute__((ext_vector_type(4))) unsigned;
using u32x2 = __attribute__((ext_vector_type(2))) unsigned;
#define KSWZ(row, colB) ((row) * 256 + ((colB) ^ (((row) & 7) << 4)))
#define SBAR() __builtin_amdgcn_sched_barrier(0)
__device__ __forceinline__ int crow(int r, int hi) { return (r & 3) + 8 * (r >> 2) + 4 * hi; }
__device__ __forceinline__ unsigned cvtpk(float lo, float hi) { unsigned r; asm volatile("v_cvt_pk_bf16_f32 %0, %1, %2" : "=v"(r) : "v"(lo), "v"(hi)); return r; }
__device__ __forceinline__ bf16x8 ld8(const bf16* p) { return *reinterpret_cast<const bf16x8*>(p); }
struct Stage { bf16x8 v0, v1, k0, k1;
  __device__ __forceinline__ void load(const bf16* V0, const bf16* V1, const bf16* K0, const bf16* K1) { v0 = ld8(V0); v1 = ld8(V1); k0 = ld8(K0); k1 = ld8(K1); } };
__device__ __forceinline__ int voff(int row, int sc) { return (row >> 5) * 8192 + 2 * ((sc >> 4) * 512 + (row & 31) * 16 + (sc & 8)); }
__device__ __forceinline__ void partialSM(f32x16& p0, f32x16& p1, float& m_reg, float& msub, float& alpha) {
  float pmax = p0[0]; for (int r = 1; r < 16; ++r) pmax = fmaxf(pmax, p0[r]); for (int r = 0; r < 16; ++r) pmax = fmaxf(pmax, p1[r]);
  { auto rr = __builtin_amdgcn_permlane32_swap(__float_as_uint(pmax), __float_as_uint(pmax), false, false);
    pmax = fmaxf(__uint_as_float(rr[0]), __uint_as_float(rr[1])); }
  pmax *= SL2E;
  if (__builtin_expect(__all(pmax - m_reg <= THR2), 1)) { alpha = 1.f; }
  else { float mn = fmaxf(m_reg, pmax); msub = (mn == -__builtin_inff()) ? 0.f : mn; alpha = __builtin_amdgcn_exp2f(m_reg - msub); m_reg = mn; }
  float nm = -msub;
  for (int r = 0; r < 16; ++r) p0[r] = fmaf(p0[r], SL2E, nm); for (int r = 0; r < 16; ++r) p1[r] = fmaf(p1[r], SL2E, nm);
  for (int r = 0; r < 16; ++r) p0[r] = __builtin_amdgcn_exp2f(p0[r]);
}
__device__ __forceinline__ void finishSM(f32x16& p0, f32x16& p1, float alpha, float& l_reg, bf16x8& pa0, bf16x8& pa1, bf16x8& pa2, bf16x8& pa3) {
  for (int r = 0; r < 16; ++r) p1[r] = __builtin_amdgcn_exp2f(p1[r]);
  float ps = 0; for (int r = 0; r < 16; ++r) ps += p0[r]; for (int r = 0; r < 16; ++r) ps += p1[r];
  { auto rr = __builtin_amdgcn_permlane32_swap(__float_as_uint(ps), __float_as_uint(ps), false, false); ps = __uint_as_float(rr[0]) + __uint_as_float(rr[1]); }
  l_reg = l_reg * alpha + ps;
#define PK4(P, BASE, OUT) do { unsigned a0 = cvtpk(P[BASE + 0], P[BASE + 1]), a1 = cvtpk(P[BASE + 2], P[BASE + 3]); \
    unsigned b0 = cvtpk(P[BASE + 4], P[BASE + 5]), b1 = cvtpk(P[BASE + 6], P[BASE + 7]); \
    auto r0 = __builtin_amdgcn_permlane32_swap(a0, b0, false, false); auto r1 = __builtin_amdgcn_permlane32_swap(a1, b1, false, false); \
    u32x4 w = {r0[0], r1[0], r0[1], r1[1]}; OUT = *reinterpret_cast<bf16x8*>(&w); } while (0)
  PK4(p0, 0, pa0); PK4(p0, 8, pa1); PK4(p1, 0, pa2); PK4(p1, 8, pa3);
#undef PK4
}
__device__ __forceinline__ void qkt(f32x16& p0, f32x16& p1, const bf16* Ks, const bf16x8* qr, int r32, int hi) {
  p0 = f32x16{}; p1 = f32x16{};
#pragma unroll
  for (int d0 = 0; d0 < 8; ++d0) { int cb = (d0 * 16 + hi * 8) * 2;
    bf16x8 b0 = *reinterpret_cast<const bf16x8*>((const char*)Ks + KSWZ(r32, cb));
    bf16x8 b1 = *reinterpret_cast<const bf16x8*>((const char*)Ks + KSWZ(32 + r32, cb));
    p0 = __builtin_amdgcn_mfma_f32_32x32x16_bf16(b0, qr[d0], p0, 0, 0, 0);
    p1 = __builtin_amdgcn_mfma_f32_32x32x16_bf16(b1, qr[d0], p1, 0, 0, 0); }
}
__device__ __forceinline__ void pv_d0(f32x16* o, int vb, bf16x8 pa0, bf16x8 pa1, bf16x8 pa2, bf16x8 pa3) {
#pragma unroll
  for (int d0 = 0; d0 < 4; ++d0) { s16x4 lo[4], hi[4];
#pragma unroll
    for (int ks = 0; ks < 4; ++ks) { int a = vb + d0 * 2048 + (ks >> 1) * 8192 + (ks & 1) * 512;
      asm volatile("ds_read_b64_tr_b16 %0,%1" : "=&v"(lo[ks]) : "v"(a) : "memory");
      asm volatile("ds_read_b64_tr_b16 %0,%1 offset:128" : "=&v"(hi[ks]) : "v"(a) : "memory"); }
    asm volatile("s_waitcnt lgkmcnt(0)" ::: "memory"); SBAR();
#define PK(k) (bf16x8){lo[k][0], lo[k][1], lo[k][2], lo[k][3], hi[k][0], hi[k][1], hi[k][2], hi[k][3]}
    o[d0] = __builtin_amdgcn_mfma_f32_32x32x16_bf16(pa0, PK(0), o[d0], 0, 0, 0);
    o[d0] = __builtin_amdgcn_mfma_f32_32x32x16_bf16(pa1, PK(1), o[d0], 0, 0, 0);
    o[d0] = __builtin_amdgcn_mfma_f32_32x32x16_bf16(pa2, PK(2), o[d0], 0, 0, 0);
    o[d0] = __builtin_amdgcn_mfma_f32_32x32x16_bf16(pa3, PK(3), o[d0], 0, 0, 0);
#undef PK
  }
}
__global__ __launch_bounds__(512, 1)
void gqa_chunk_fwd(const bf16* __restrict__ Q, const bf16* __restrict__ K, const bf16* __restrict__ V, float* __restrict__ pml, float* __restrict__ po) {
  constexpr int NS = NSPLIT;
  const int tid = threadIdx.x, wid = tid >> 6, lane = tid & 63, r32 = lane & 31, hi = lane >> 5;
  const int split = blockIdx.x, kvh = blockIdx.y, b = blockIdx.z;
  const bf16* Qg = Q + ((size_t)b * HQ + (size_t)kvh * G) * SQ * D;
  const bf16* Kh = K + (((size_t)b * HKV + kvh) * SK + (size_t)split * TPS * KVBLK) * D;
  const bf16* Vh = V + (((size_t)b * HKV + kvh) * SK + (size_t)split * TPS * KVBLK) * D;
  extern __shared__ __attribute__((aligned(16))) char shm[];
  bf16* V_lds = (bf16*)shm; bf16* K_lds = (bf16*)(shm + 2 * SHM_V);
  float* ws = (float*)(shm + SHM_WS) + wid * 64; float* al_l = ws + 32;
  const int R = wid * QBLK + r32;
  bf16x8 qr[8]; const bf16* qrow = Qg + (size_t)R * D;
  float m_reg = -__builtin_inff(), msub = 0.f, l_reg = 0.f; f32x16 o[4] = {};
  const int sr = tid >> 4, sc = (tid & 15) * 8;
  const int vb0 = (int)(uintptr_t)V_lds + (lane & 15) * 8 + ((lane >> 4) & 1) * 1024 + (lane >> 5) * 256;
  Stage st;
#define SLOAD(k0) do { st.load(Vh + (size_t)((k0) + sr) * D + sc, Vh + (size_t)((k0) + 32 + sr) * D + sc, Kh + (size_t)((k0) + sr) * D + sc, Kh + (size_t)((k0) + 32 + sr) * D + sc); } while (0)
#define SWRITE(buf) do { \
      *(bf16x8*)((char*)V_lds + (buf) * SHM_V + voff(sr, sc)) = st.v0; *(bf16x8*)((char*)V_lds + (buf) * SHM_V + voff(32 + sr, sc)) = st.v1; int kc = sc * 2; \
      *(bf16x8*)((char*)K_lds + (buf) * SHM_K + KSWZ(sr, kc)) = st.k0; *(bf16x8*)((char*)K_lds + (buf) * SHM_K + KSWZ(32 + sr, kc)) = st.k1; } while (0)
#define RESC(a) do { if (__any((a) < 1.f)) { if (hi == 0) al_l[r32] = (a); asm volatile("s_waitcnt lgkmcnt(0)" ::: "memory"); \
      for (int d = 0; d < 4; ++d) for (int r = 0; r < 16; ++r) o[d][r] *= al_l[crow(r, hi)]; } } while (0)
  f32x16 pA0, pA1, pB0, pB1; float alA, alB; bf16x8 pa0, pa1, pa2, pa3;
  for (int d0 = 0; d0 < 8; ++d0) qr[d0] = ld8(qrow + d0 * 16 + hi * 8);
  SLOAD(0); SWRITE(0); __syncthreads();
  qkt(pA0, pA1, K_lds, qr, r32, hi); partialSM(pA0, pA1, m_reg, msub, alA);
  SLOAD(KVBLK); SWRITE(1); __syncthreads();
  for (int j = 1; j + 1 < TPS; j += 2) {
    SBAR(); qkt(pB0, pB1, (bf16*)((char*)K_lds + SHM_K), qr, r32, hi);
    finishSM(pA0, pA1, alA, l_reg, pa0, pa1, pa2, pa3); SBAR();
    SLOAD((j + 1) * KVBLK); SBAR();
    pv_d0(o, vb0, pa0, pa1, pa2, pa3); partialSM(pB0, pB1, m_reg, msub, alB);
    __syncthreads(); asm volatile("s_waitcnt vmcnt(0)" ::: "memory"); SWRITE(0);
    RESC(alB); __syncthreads();
    SBAR(); qkt(pA0, pA1, K_lds, qr, r32, hi);
    finishSM(pB0, pB1, alB, l_reg, pa0, pa1, pa2, pa3); SBAR();
    if (j + 2 < TPS) { SLOAD((j + 2) * KVBLK); SBAR(); }
    pv_d0(o, vb0 + (int)SHM_V, pa0, pa1, pa2, pa3); partialSM(pA0, pA1, m_reg, msub, alA);
    __syncthreads(); if (j + 2 < TPS) { asm volatile("s_waitcnt vmcnt(0)" ::: "memory"); SWRITE(1); }
    RESC(alA); __syncthreads();
  }
  SBAR(); qkt(pB0, pB1, (bf16*)((char*)K_lds + SHM_K), qr, r32, hi);
  finishSM(pA0, pA1, alA, l_reg, pa0, pa1, pa2, pa3); SBAR();
  pv_d0(o, vb0, pa0, pa1, pa2, pa3); partialSM(pB0, pB1, m_reg, msub, alB);
  __syncthreads(); RESC(alB);
  finishSM(pB0, pB1, alB, l_reg, pa0, pa1, pa2, pa3); SBAR();
  pv_d0(o, vb0 + (int)SHM_V, pa0, pa1, pa2, pa3);
  const size_t row0 = ((size_t)b * HQ + (size_t)kvh * G) * SQ;
  if (hi == 0) { float* p = pml + ((row0 + R) * NS + split) * 2; p[0] = m_reg; p[1] = l_reg; }
#pragma unroll
  for (int r = 0; r < 16; ++r) { const int Ro = wid * QBLK + crow(r, hi); float* prow = po + ((row0 + Ro) * NS + split) * D;
      for (int d0 = 0; d0 < 4; ++d0) prow[d0 * 32 + r32] = o[d0][r]; }
}
__device__ __forceinline__ float bf2f(unsigned short x) { return __uint_as_float((unsigned)x << 16); }
constexpr int CMB_ROWS = 8, CMB_BLOCKS = (int)(((size_t)B * HQ * SQ + CMB_ROWS - 1) / CMB_ROWS);
__global__ __launch_bounds__(256) void gqa_chunk_combine(const float* __restrict__ pml, const float* __restrict__ po,
   const unsigned short* __restrict__ Q, const unsigned short* __restrict__ KT, const unsigned short* __restrict__ VT, const unsigned short* __restrict__ TM, unsigned short* __restrict__ O) {
  constexpr int NS = NSPLIT;
  const int row = blockIdx.x * CMB_ROWS + (threadIdx.x >> 5); const int l = threadIdx.x & 31, d = l * 4;
  const int t = row & 31, h = (row >> 5) & 31, b = row >> 10, kvh = h >> 3;
  const unsigned short* q = Q + (size_t)row * D; const unsigned short* kt = KT + ((size_t)(b * HKV + kvh) * SQ + l) * D;
  float s = 0.f;
  for (int i = 0; i < D; i += 8) { bf16x8 a = *reinterpret_cast<const bf16x8*>(q + i), c = *reinterpret_cast<const bf16x8*>(kt + i);
    for (int j = 0; j < 8; ++j) s = fmaf(bf2f((unsigned short)a[j]), bf2f((unsigned short)c[j]), s); }
  const float mk = bf2f(TM[(b * SQ + t) * SQ + l]);
  s = (s * SCALE + (1.f - mk) * -1000000000.0f) * L2E;
  float mt = s; for (int o = 16; o; o >>= 1) mt = fmaxf(mt, __shfl_xor(mt, o, 32));
  const float* ml = pml + (size_t)row * NS * 2; const float* acc = po + (size_t)row * NS * D + d;
  float M = mt; for (int sp = 0; sp < NS; ++sp) M = fmaxf(M, ml[2 * sp]);
  float p = __builtin_amdgcn_exp2f(s - M);
  float L = p; for (int o = 16; o; o >>= 1) L += __shfl_xor(L, o, 32);
  f32x4 o = {0.f, 0.f, 0.f, 0.f};
  const unsigned short* vt = VT + (size_t)(b * HKV + kvh) * SQ * D + d;
  for (int i = 0; i < 32; ++i) { const float pi = __shfl(p, i, 32); s16x4 v = *reinterpret_cast<const s16x4*>(vt + i * D);
    for (int j = 0; j < 4; ++j) o[j] = fmaf(pi, bf2f((unsigned short)v[j]), o[j]); }
  for (int sp = 0; sp < NS; ++sp) { const float m = ml[2 * sp], l_ = ml[2 * sp + 1]; const float w = __builtin_amdgcn_exp2f(m - M);
    const f32x4 a = *reinterpret_cast<const f32x4*>(acc + (size_t)sp * D); L += w * l_; o[0] += w * a[0]; o[1] += w * a[1]; o[2] += w * a[2]; o[3] += w * a[3]; }
  const float inv = 1.f / L;
  u32x2 w = {cvtpk(o[0] * inv, o[1] * inv), cvtpk(o[2] * inv, o[3] * inv)};
  *reinterpret_cast<u32x2*>(O + (size_t)row * D + d) = w;
}
extern "C" void kernel_launch(void* const* d_in, const int* in_sizes, int n_in, void* d_out, int out_size, void* d_ws, size_t ws_size, hipStream_t stream) {
  hipFuncSetAttribute((const void*)gqa_chunk_fwd, hipFuncAttributeMaxDynamicSharedMemorySize, (int)SHM_TOT);
  float* pml = (float*)((char*)d_ws + WS_PML); float* po = (float*)((char*)d_ws + WS_PO);
  hipLaunchKernelGGL(gqa_chunk_fwd, dim3(NSPLIT, HKV, B), dim3(512), SHM_TOT, stream, (const bf16*)d_in[0], (const bf16*)d_in[1], (const bf16*)d_in[2], pml, po);
  hipLaunchKernelGGL(gqa_chunk_combine, dim3(CMB_BLOCKS), dim3(256), 0, stream, (const float*)pml, (const float*)po,
     (const unsigned short*)d_in[0], (const unsigned short*)d_in[3], (const unsigned short*)d_in[4], (const unsigned short*)d_in[5], (unsigned short*)d_out);
}
```

```cpp
#include <hip/hip_runtime.h>

__global__ __launch_bounds__(256, 1)
void tree_attn(const void* Qp, const void* Kp, const void* Vp, const void* KTp, const void* VTp, const void* TMp, void* ws, void* out) {
  unsigned tid = threadIdx.x;
  asm volatile(
    "s_load_dwordx16 s[8:23], %[ka], 0x0\n\t"
    "v_lshrrev_b32 v249, 6, %[tid]\n\t"
    "v_and_b32 v231, 63, %[tid]\n\t"
    "s_and_b32 s71, %[blk], 31\n\t"
    "s_lshr_b32 s72, %[blk], 5\n\t"
    "v_readfirstlane_b32 s70, v249\n\t"
    "s_mov_b32 s67, 0\n\t"
    ".Lpass%=:\n\t"
    "v_and_b32 v250, 15, v231\n\t"
    "v_lshrrev_b32 v251, 4, v231\n\t"
    "s_getpc_b64 s[62:63]\n\t"
    "s_lshl_b32 s64, s72, 12\n\t"
    "s_lshl_b32 s65, s70, 10\n\t"
    "s_add_u32 s64, s64, s65\n\t"
    "v_and_b32 v248, 7, v231\n\t"
    "s_min_u32 s64, s64, 0x4c00\n\t"
    "v_lshl_add_u32 v248, v248, 7, s64\n\t"
    "global_load_dword v254, v248, s[62:63]\n\t"
    "s_lshl_b32 s73, s70, 12\n\t"
    "s_lshl_b32 s74, s70, 11\n\t"
    "v_xor_b32 v248, v250, v251\n\t"
    "v_lshlrev_b32 v248, 4, v248\n\t"
    "v_lshl_or_b32 v232, v250, 8, v248\n\t"
    "v_lshl_or_b32 v248, v251, 8, v248\n\t"
    "v_or_b32 v224, s73, v248\n\t"
    "v_xor_b32 v225, 64, v224\n\t"
    "v_xor_b32 v226, 128, v224\n\t"
    "v_xor_b32 v227, 192, v224\n\t"
    "s_and_b32 s64, s70, 1\n\t"
    "s_lshl_b32 s75, s64, 12\n\t"
    "s_lshr_b32 s65, s70, 1\n\t"
    "s_lshl_b32 s65, s65, 10\n\t"
    "s_or_b32 s75, s75, s65\n\t"
    "s_lshl_b32 s64, s64, 7\n\t"
    "s_or_b32 s64, s64, s75\n\t"
    "v_xor_b32 v188, s64, v248\n\t"
    "v_xor_b32 v189, 64, v188\n\t"
    "v_add_u32 v189, 0x400, v189\n\t"
    "v_lshlrev_b32 v255, 4, v251\n\t"
    "v_lshl_or_b32 v255, v250, 6, v255\n\t"
    "s_add_u32 s76, s72, 0\n\t"
    "s_and_b32 s76, s76, 3\n\t"
    "s_lshl_b32 s76, s76, 12\n\t"
    "s_add_u32 s77, s72, 1\n\t"
    "s_and_b32 s77, s77, 3\n\t"
    "s_lshl_b32 s77, s77, 12\n\t"
    "s_add_u32 s78, s72, 2\n\t"
    "s_and_b32 s78, s78, 3\n\t"
    "s_lshl_b32 s78, s78, 12\n\t"
    "s_add_u32 s79, s72, 3\n\t"
    "s_and_b32 s79, s79, 3\n\t"
    "s_lshl_b32 s79, s79, 12\n\t"
    "s_lshr_b32 s88, s76, 2\n\t"
    "s_and_b32 s88, s88, 0x400\n\t"
    "s_lshr_b32 s89, s77, 2\n\t"
    "s_and_b32 s89, s89, 0x400\n\t"
    "s_and_b32 s64, s72, 1\n\t"
    "s_lshl_b32 s64, s64, 1\n\t"
    "s_lshr_b32 s65, s70, 1\n\t"
    "s_or_b32 s52, s64, s65\n\t"
    "v_bfe_u32 v246, v231, 1, 2\n\t"
    "v_sub_u32 v246, s52, v246\n\t"
    "v_and_b32 v246, 3, v246\n\t"
    "v_and_b32 v245, 1, v231\n\t"
    "v_lshl_or_b32 v246, v245, 2, v246\n\t"
    "v_cmp_eq_u32 vcc, s72, v246\n\t"
    "v_cmp_eq_u32 s[68:69], s52, v246\n\t"
    "s_xor_b32 s64, s72, s52\n\t"
    "s_or_b64 vcc, vcc, s[68:69]\n\t"
    "v_xor_b32 v245, s64, v246\n\t"
    "v_cndmask_b32 v246, v246, v245, vcc\n\t"
    "s_mul_i32 s64, %[blk], 256\n\t"
    "s_lshl_b32 s65, s70, 5\n\t"
    "s_add_u32 s64, s64, s65\n\t"
    "v_mul_u32_u24 v245, 8192, v246\n\t"
    "v_lshl_add_u32 v246, v246, 2, s64\n\t"
    "s_movk_i32 s54, 0x3e00\n\t"
    "s_cmp_lg_u32 s67, 0\n\t"
    "s_cselect_b32 s54, s54, 0x3e03\n\t"
    "s_cselect_b32 s55, 0, 0xb9d8\n\t"
    "s_cselect_b32 s53, 0x3f8293ee, 1.0\n\t"
    "s_mul_i32 s64, s70, 12288\n\t"
    "s_lshl_b32 s65, s71, 16\n\t"
    "s_add_u32 s64, s64, s65\n\t"
    "s_add_u32 s65, s73, 0x4000\n\t"
    "s_lshl_b32 m0, s72, 1\n\t"
    "s_lshl_b32 s62, s71, 21\n\t"
    "s_sub_u32 s63, s72, 1\n\t"
    "s_cmp_lt_u32 s63, 2\n\t"
    "s_cselect_b32 s62, s62, 0\n\t"
    "s_cmp_eq_u32 s72, 6\n\t"
    "s_cselect_b32 s62, 0x2100000, s62\n\t"
    "v_mov_b32 v128, s62\n\t"
    "s_waitcnt lgkmcnt(0)\n\t"
    "s_movrels_b64 s[62:63], s[8:9]\n\t"
    "global_load_dword v254, v128, s[62:63]\n\t"
    "s_add_u32 s24, s8, s64\n\t"
    "s_addc_u32 s25, s9, 0\n\t"
    "s_and_b32 s25, s25, 0xffff\n\t"
    "s_mov_b32 s26, s65\n\t"
    "s_mov_b32 s27, 0x20000\n\t"
    "s_add_u32 m0, s73, 0x18000\n\t"
    "s_nop 0\n\t"
    "buffer_load_dwordx4 v224, s[24:27], s78 offen lds\n\t"
    "s_lshl_b32 s64, s71, 13\n\t"
    "s_add_u32 s36, s14, s64\n\t"
    "s_addc_u32 s37, s15, 0\n\t"
    "s_and_b32 s37, s37, 0xffff\n\t"
    "s_mov_b32 s38, 0x2000\n\t"
    "buffer_load_dwordx4 v225, s[24:27], s78 offen offset:1024 lds\n\t"
    "s_mov_b32 s39, 0x20000\n\t"
    "s_add_u32 s40, s16, s64\n\t"
    "s_addc_u32 s41, s17, 0\n\t"
    "s_and_b32 s41, s41, 0xffff\n\t"
    "buffer_load_dwordx4 v226, s[24:27], s78 offen offset:2048 lds\n\t"
    "s_mov_b32 s42, 0x2000\n\t"
    "s_mov_b32 s43, 0x20000\n\t"
    "s_lshr_b32 s64, s71, 2\n\t"
    "s_lshl_b32 s64, s64, 11\n\t"
    "s_add_u32 s44, s18, s64\n\t"
    "buffer_load_dwordx4 v227, s[24:27], s78 offen offset:3072 lds\n\t"
    "s_addc_u32 s45, s19, 0\n\t"
    "s_and_b32 s45, s45, 0xffff\n\t"
    "s_mov_b32 s46, 0x800\n\t"
    "s_mov_b32 s47, 0x20000\n\t"
    "s_add_u32 m0, s74, 0xc000\n\t"
    "s_nop 0\n\t"
    "buffer_load_dwordx4 v188, s[36:39], 0 offen lds\n\t"
    "s_lshl_b32 s64, s71, 21\n\t"
    "s_lshl_b32 s65, s72, 18\n\t"
    "s_add_u32 s64, s64, s65\n\t"
    "s_add_u32 s28, s10, s64\n\t"
    "s_addc_u32 s29, s11, 0\n\t"
    "s_and_b32 s29, s29, 0xffff\n\t"
    "s_mov_b32 s30, 0x40000\n\t"
    "buffer_load_dwordx4 v189, s[36:39], 0 offen offset:1024 lds\n\t"
    "s_mov_b32 s31, 0x20000\n\t"
    "s_add_u32 s32, s12, s64\n\t"
    "s_addc_u32 s33, s13, 0\n\t"
    "s_and_b32 s33, s33, 0xffff\n\t"
    "s_mov_b32 s34, 0x40000\n\t"
    "s_mov_b32 s35, 0x20000\n\t"
    "s_mov_b32 s64, 0x2000000\n\t"
    "buffer_load_dwordx4 v[160:163], v255, s[44:47], s88 offen\n\t"
    "s_add_u32 s56, s20, s64\n\t"
    "s_addc_u32 s57, s21, 0\n\t"
    "s_and_b32 s57, s57, 0xffff\n\t"
    "s_mov_b32 s58, 65536\n\t"
    "s_mov_b32 s59, 0x20000\n\t"
    "v_lshlrev_b32 v248, 1, v251\n\t"
    "v_xor_b32 v248, v250, v248\n\t"
    "buffer_load_dwordx4 v[164:167], v255, s[44:47], s89 offen\n\t"
    "v_lshlrev_b32 v248, 4, v248\n\t"
    "v_lshl_or_b32 v248, v251, 8, v248\n\t"
    "v_or_b32 v228, s73, v248\n\t"
    "v_xor_b32 v229, 0x80, v228\n\t"
    "v_or_b32 v190, s75, v248\n\t"
    "v_xor_b32 v191, 0x80, v190\n\t"
    "v_add_u32 v191, 0x400, v191\n\t"
    "s_cmp_lg_u32 s67, 0\n\t"
    "s_cbranch_scc1 .Lreq2%=\n\t"
    "buffer_load_dword v230, v246, s[56:59], 0 offen sc1\n\t"
    ".Lreq1%=:\n\t"
    "v_xor_b32 v233, 64, v232\n\t"
    "v_xor_b32 v234, 128, v232\n\t"
    "v_xor_b32 v235, 192, v232\n\t"
    "v_add_u32 v140, s73, v232\n\t"
    "v_add_u32 v141, s73, v233\n\t"
    "v_add_u32 v142, s73, v234\n\t"
    "s_mov_b32 s60, 0x0\n\t"
    "s_add_u32 m0, s73, 0x0\n\t"
    "s_nop 0\n\t"
    "buffer_load_dwordx4 v224, s[28:31], s60 offen nt lds\n\t"
    "v_add_u32 v143, s73, v235\n\t"
    "v_add_u32 v136, 0x18000, v140\n\t"
    "v_add_u32 v137, 0x18000, v141\n\t"
    "v_add_u32 v138, 0x18000, v142\n\t"
    "v_add_u32 v139, 0x18000, v143\n\t"
    "v_lshrrev_b32 v128, 2, v250\n\t"
    "buffer_load_dwordx4 v225, s[28:31], s60 offen offset:1024 nt lds\n\t"
    "v_cmp_eq_u32 vcc, v128, v251\n\t"
    "v_and_b32 v128, 1, v250\n\t"
    "v_lshlrev_b32 v128, 4, v128\n\t"
    "v_mov_b32 v129, s54\n\t"
    "v_mov_b32 v130, s55\n\t"
    "s_add_u32 m0, s73, 0x8000\n\t"
    "s_nop 0\n\t"
    "buffer_load_dwordx4 v224, s[24:27], s79 offen lds\n\t"
    "v_lshlrev_b32 v129, v128, v129\n\t"
    "v_lshlrev_b32 v130, v128, v130\n\t"
    "v_cndmask_b32 v129, 0, v129, vcc\n\t"
    "v_cndmask_b32 v130, 0, v130, vcc\n\t"
    "v_and_b32 v128, 2, v250\n\t"
    "v_cmp_eq_u32 vcc, 0, v128\n\t"
    "buffer_load_dwordx4 v225, s[24:27], s79 offen offset:1024 lds\n\t"
    "v_accvgpr_write_b32 a228, 0\n\t"
    "v_accvgpr_write_b32 a229, 0\n\t"
    "v_accvgpr_write_b32 a232, 0\n\t"
    "v_accvgpr_write_b32 a233, 0\n\t"
    "v_accvgpr_write_b32 a236, 0\n\t"
    "buffer_load_dwordx4 v226, s[24:27], s79 offen offset:2048 lds\n\t"
    "v_accvgpr_write_b32 a237, 0\n\t"
    "v_accvgpr_write_b32 a238, 0\n\t"
    "v_accvgpr_write_b32 a239, 0\n\t"
    "v_cndmask_b32 v131, 0, v129, vcc\n\t"
    "v_cndmask_b32 v132, 0, v130, vcc\n\t"
    "v_sub_u32 v129, v129, v131\n\t"
    "buffer_load_dwordx4 v227, s[24:27], s79 offen offset:3072 lds\n\t"
    "v_sub_u32 v130, v130, v132\n\t"
    "v_accvgpr_write_b32 a230, v131\n\t"
    "v_accvgpr_write_b32 a231, v129\n\t"
    "v_accvgpr_write_b32 a234, v132\n\t"
    "v_accvgpr_write_b32 a235, v130\n\t"
    "v_lshrrev_b32 v248, 2, v231\n\t"
    "v_and_b32 v244, 7, v248\n\t"
    "v_lshlrev_b32 v244, 5, v244\n\t"
    "v_lshl_or_b32 v248, v248, 8, v244\n\t"
    "v_and_b32 v244, 3, v231\n\t"
    "v_lshl_or_b32 v248, v244, 3, v248\n\t"
    "v_or_b32 v236, 0x10000, v248\n\t"
    "v_xor_b32 v237, 32, v236\n\t"
    "v_xor_b32 v238, 64, v236\n\t"
    "v_xor_b32 v239, 96, v236\n\t"
    "v_xor_b32 v240, 128, v236\n\t"
    "v_mfma_f32_32x32x16_bf16 v[0:15], a[236:239], a[236:239], 0\n\t"
    "v_xor_b32 v241, 160, v236\n\t"
    "v_xor_b32 v242, 192, v236\n\t"
    "v_xor_b32 v243, 224, v236\n\t"
    "v_lshlrev_b32 v244, 4, v231\n\t"
    "s_mul_i32 s60, s71, 256\n\t"
    "s_lshl_b32 s65, s72, 4\n\t"
    "s_add_u32 s64, s60, s65\n\t"
    "s_lshl_b32 s65, s70, 2\n\t"
    "s_add_u32 s64, s64, s65\n\t"
    "v_add_u32 v176, s64, v245\n\t"
    "v_mfma_f32_32x32x16_bf16 v[16:31], a[236:239], a[236:239], 0\n\t"
    "v_bfe_u32 v247, v231, 2, 1\n\t"
    "v_mul_u32_u24 v247, 8192, v247\n\t"
    "v_and_b32 v245, 3, v231\n\t"
    "v_lshl_add_u32 v247, v245, 5, v247\n\t"
    "s_mul_i32 s65, s70, 16384\n\t"
    "s_add_u32 s64, s60, s65\n\t"
    "s_lshl_b32 s65, s72, 2\n\t"
    "s_add_u32 s64, s64, s65\n\t"
    "v_add_u32 v247, s64, v247\n\t"
    "v_lshlrev_b32 v245, 3, v250\n\t"
    "s_lshl_b32 s64, s71, 6\n\t"
    "v_mfma_f32_32x32x16_bf16 v[32:47], a[236:239], a[236:239], 0\n\t"
    "s_lshl_b32 s65, s70, 4\n\t"
    "s_add_u32 s64, s64, s65\n\t"
    "s_add_u32 s64, s64, s72\n\t"
    "s_mul_i32 s64, s64, 8448\n\t"
    "s_add_u32 s48, s20, s64\n\t"
    "s_addc_u32 s49, s21, 0\n\t"
    "s_and_b32 s49, s49, 0xffff\n\t"
    "s_mov_b32 s50, 76032\n\t"
    "s_mov_b32 s51, 0x20000\n\t"
    "v_add_u32 v245, 0x2000, v245\n\t"
    "v_mfma_f32_32x32x16_bf16 v[48:63], a[236:239], a[236:239], 0\n\t"
    "s_lshr_b32 s80, s76, 13\n\t"
    "s_lshl_b32 s64, s70, 1\n\t"
    "s_add_u32 s64, s64, s80\n\t"
    "s_mul_i32 s80, s80, 67584\n\t"
    "s_cmp_eq_u32 s64, s72\n\t"
    "s_cselect_b32 s80, 0x40000000, s80\n\t"
    "s_and_b32 s64, s76, 0x1000\n\t"
    "s_lshr_b32 s65, s64, 5\n\t"
    "s_add_u32 s65, s80, s65\n\t"
    "s_lshl_b64 exec, 0xffff, 0\n\t"
    "v_add_u32 v245, s65, v245\n\t"
    "s_mov_b64 exec, -1\n\t"
    "s_add_u32 s80, s80, s64\n\t"
    "v_mfma_f32_32x32x16_bf16 v[64:79], a[236:239], a[236:239], 0\n\t"
    "s_lshr_b32 s64, s76, 11\n\t"
    "s_lshl_b32 s84, 3, s64\n\t"
    "s_lshr_b32 s81, s77, 13\n\t"
    "s_lshl_b32 s64, s70, 1\n\t"
    "s_add_u32 s64, s64, s81\n\t"
    "s_mul_i32 s81, s81, 67584\n\t"
    "s_cmp_eq_u32 s64, s72\n\t"
    "s_cselect_b32 s81, 0x40000000, s81\n\t"
    "s_and_b32 s64, s77, 0x1000\n\t"
    "s_lshr_b32 s65, s64, 5\n\t"
    "v_mfma_f32_32x32x16_bf16 v[80:95], a[236:239], a[236:239], 0\n\t"
    "s_add_u32 s65, s81, s65\n\t"
    "s_lshl_b64 exec, 0xffff, 16\n\t"
    "v_add_u32 v245, s65, v245\n\t"
    "s_mov_b64 exec, -1\n\t"
    "s_add_u32 s81, s81, s64\n\t"
    "s_lshr_b32 s64, s77, 11\n\t"
    "s_lshl_b32 s85, 3, s64\n\t"
    "s_lshr_b32 s82, s78, 13\n\t"
    "s_lshl_b32 s64, s70, 1\n\t"
    "s_add_u32 s64, s64, s82\n\t"
    "s_mul_i32 s82, s82, 67584\n\t"
    "s_cmp_eq_u32 s64, s72\n\t"
    "s_cselect_b32 s82, 0x40000000, s82\n\t"
    "v_mfma_f32_32x32x16_bf16 v[96:111], a[236:239], a[236:239], 0\n\t"
    "s_and_b32 s64, s78, 0x1000\n\t"
    "s_lshr_b32 s65, s64, 5\n\t"
    "s_add_u32 s65, s82, s65\n\t"
    "s_lshl_b64 exec, 0xffff, 32\n\t"
    "v_add_u32 v245, s65, v245\n\t"
    "s_mov_b64 exec, -1\n\t"
    "s_add_u32 s82, s82, s64\n\t"
    "s_lshr_b32 s64, s78, 11\n\t"
    "s_lshl_b32 s86, 3, s64\n\t"
    "s_lshr_b32 s83, s79, 13\n\t"
    "s_lshl_b32 s64, s70, 1\n\t"
    "s_add_u32 s64, s64, s83\n\t"
    "v_mfma_f32_32x32x16_bf16 v[112:127], a[236:239], a[236:239], 0\n\t"
    "s_mul_i32 s83, s83, 67584\n\t"
    "s_cmp_eq_u32 s64, s72\n\t"
    "s_cselect_b32 s83, 0x40000000, s83\n\t"
    "s_and_b32 s64, s79, 0x1000\n\t"
    "s_lshr_b32 s65, s64, 5\n\t"
    "s_add_u32 s65, s83, s65\n\t"
    "s_lshl_b64 exec, 0xffff, 48\n\t"
    "v_add_u32 v245, s65, v245\n\t"
    "s_mov_b64 exec, -1\n\t"
    "s_add_u32 s83, s83, s64\n\t"
    "s_lshr_b32 s64, s79, 11\n\t"
    "s_lshl_b32 s87, 3, s64\n\t"
    "s_and_b32 s64, s72, 5\n\t"
    "v_mfma_f32_32x32x16_bf16 a[192:207], a[236:239], a[236:239], 0\n\t"
    "s_cselect_b32 s75, 0x7f800000, 0\n\t"
    "s_mov_b32 s66, 0xceabfb8f\n\t"
    "s_cmp_lg_u32 s67, 0\n\t"
    "s_cselect_b32 s66, 0xcea8965f, s66\n\t"
    "s_mov_b32 s61, 0x41400000\n\t"
    "v_mov_b32 v249, 0x3f803f80\n\t"
    "v_accvgpr_write_b32 a208, v249\n\t"
    "v_accvgpr_write_b32 a209, v249\n\t"
    "v_accvgpr_write_b32 a210, v249\n\t"
    "v_accvgpr_write_b32 a211, v249\n\t"
    "v_mfma_f32_32x32x16_bf16 a[128:143], a[236:239], a[236:239], 0\n\t"
    "v_mov_b32 v212, 0xff800000\n\t"
    "s_waitcnt vmcnt(11)\n\t"
    "ds_read_b128 a[32:35], v136 offset:0\n\t"
    "s_add_u32 m0, s73, 0x20000\n\t"
    "s_nop 0\n\t"
    "buffer_load_dwordx4 v224, s[24:27], s76 offen lds\n\t"
    "ds_read_b128 a[36:39], v137 offset:0\n\t"
    "buffer_load_dwordx4 v225, s[24:27], s76 offen offset:1024 lds\n\t"
    "ds_read_b128 a[40:43], v138 offset:0\n\t"
    "buffer_load_dwordx4 v226, s[24:27], s76 offen offset:2048 lds\n\t"
    "ds_read_b128 a[44:47], v139 offset:0\n\t"
    "buffer_load_dwordx4 v227, s[24:27], s76 offen offset:3072 lds\n\t"
    "s_waitcnt vmcnt(11)\n\t"
    "s_barrier\n\t"
    "s_memrealtime s[96:97]\n\t"
    "ds_read_b128 a[96:99], v232 offset:49152\n\t"
    "ds_read_b128 a[100:103], v233 offset:49152\n\t"
    "ds_read_b128 a[104:107], v234 offset:49152\n\t"
    "ds_read_b128 a[108:111], v235 offset:49152\n\t"
    "ds_read_b128 a[112:115], v232 offset:53248\n\t"
    "ds_read_b128 a[116:119], v233 offset:53248\n\t"
    "ds_read_b128 a[120:123], v234 offset:53248\n\t"
    "ds_read_b128 a[124:127], v235 offset:53248\n\t"
    "s_waitcnt lgkmcnt(0)\n\t"
    "v_mfma_f32_16x16x32_bf16 v[128:131], a[230:233], a[32:35], 0\n\t"
    "s_add_u32 m0, s73, 0x24000\n\t"
    "s_nop 0\n\t"
    "buffer_load_dwordx4 v224, s[24:27], s77 offen lds\n\t"
    "buffer_load_dwordx4 v225, s[24:27], s77 offen offset:1024 lds\n\t"
    "v_lshlrev_b32 v144, 16, v160\n\t"
    "v_and_b32 v145, 0xffff0000, v160\n\t"
    "v_mfma_f32_16x16x32_bf16 v[132:135], a[228:231], a[32:35], 0\n\t"
    "v_lshlrev_b32 v146, 16, v161\n\t"
    "v_and_b32 v147, 0xffff0000, v161\n\t"
    "v_lshlrev_b32 v148, 16, v162\n\t"
    "v_and_b32 v149, 0xffff0000, v162\n\t"
    "v_lshlrev_b32 v150, 16, v163\n\t"
    "v_mfma_f32_16x16x32_bf16 v[128:131], a[234:237], a[32:35], v[128:131]\n\t"
    "v_and_b32 v151, 0xffff0000, v163\n\t"
    "v_lshlrev_b32 v152, 16, v164\n\t"
    "v_and_b32 v153, 0xffff0000, v164\n\t"
    "v_lshlrev_b32 v154, 16, v165\n\t"
    "v_mfma_f32_16x16x32_bf16 v[132:135], a[232:235], a[32:35], v[132:135]\n\t"
    "v_and_b32 v155, 0xffff0000, v165\n\t"
    "v_lshlrev_b32 v156, 16, v166\n\t"
    "v_and_b32 v157, 0xffff0000, v166\n\t"
    "v_lshlrev_b32 v158, 16, v167\n\t"
    "v_and_b32 v159, 0xffff0000, v167\n\t"
    "v_mfma_f32_16x16x32_bf16 v[168:171], a[230:233], a[36:39], 0\n\t"
    "v_cvt_pk_bf16_f32 v128, v128, v129\n\t"
    "buffer_load_dwordx4 v226, s[24:27], s77 offen offset:2048 lds\n\t"
    "buffer_load_dwordx4 v227, s[24:27], s77 offen offset:3072 lds\n\t"
    "v_mfma_f32_16x16x32_bf16 v[172:175], a[228:231], a[36:39], 0\n\t"
    "v_cvt_pk_bf16_f32 v130, v130, v131\n\t"
    "v_fma_f32 v144, v144, -s66, s66\n\t"
    "v_fma_f32 v145, v145, -s66, s66\n\t"
    "v_fma_f32 v146, v146, -s66, s66\n\t"
    "v_mfma_f32_16x16x32_bf16 v[168:171], a[234:237], a[36:39], v[168:171]\n\t"
    "v_cvt_pk_bf16_f32 v132, v132, v133\n\t"
    "v_fma_f32 v147, v147, -s66, s66\n\t"
    "v_fma_f32 v148, v148, -s66, s66\n\t"
    "v_fma_f32 v149, v149, -s66, s66\n\t"
    "v_mfma_f32_16x16x32_bf16 v[172:175], a[232:235], a[36:39], v[172:175]\n\t"
    "v_cvt_pk_bf16_f32 v134, v134, v135\n\t"
    "v_fma_f32 v150, v150, -s66, s66\n\t"
    "v_fma_f32 v151, v151, -s66, s66\n\t"
    "v_fma_f32 v152, v152, -s66, s66\n\t"
    "v_accvgpr_write_b32 a32, v128\n\t"
    "v_accvgpr_write_b32 a33, v130\n\t"
    "v_fma_f32 v153, v153, -s66, s66\n\t"
    "v_fma_f32 v154, v154, -s66, s66\n\t"
    "v_fma_f32 v155, v155, -s66, s66\n\t"
    "v_accvgpr_write_b32 a34, v132\n\t"
    "v_accvgpr_write_b32 a35, v134\n\t"
    "v_fma_f32 v156, v156, -s66, s66\n\t"
    "v_fma_f32 v157, v157, -s66, s66\n\t"
    "v_fma_f32 v158, v158, -s66, s66\n\t"
    "v_fma_f32 v159, v159, -s66, s66\n\t"
    "v_mfma_f32_16x16x32_bf16 v[128:131], a[230:233], a[40:43], 0\n\t"
    "v_cvt_pk_bf16_f32 v168, v168, v169\n\t"
    "v_mfma_f32_16x16x32_bf16 v[132:135], a[228:231], a[40:43], 0\n\t"
    "v_cvt_pk_bf16_f32 v170, v170, v171\n\t"
    "s_waitcnt vmcnt(8)\n\t"
    "v_mfma_f32_16x16x32_bf16 v[128:131], a[234:237], a[40:43], v[128:131]\n\t"
    "v_cvt_pk_bf16_f32 v172, v172, v173\n\t"
    "ds_read_b128 a[48:51], v140 offset:32768\n\t"
    "v_mfma_f32_16x16x32_bf16 v[132:135], a[232:235], a[40:43], v[132:135]\n\t"
    "v_cvt_pk_bf16_f32 v174, v174, v175\n\t"
    "ds_read_b128 a[52:55], v141 offset:32768\n\t"
    "ds_read_b128 a[56:59], v142 offset:32768\n\t"
    "v_mfma_f32_16x16x32_bf16 v[192:195], a[96:99], a[32:35], v[144:147]\n\t"
    "v_accvgpr_write_b32 a36, v168\n\t"
    "v_accvgpr_write_b32 a37, v170\n\t"
    "ds_read_b128 a[60:63], v143 offset:32768\n\t"
    "v_mfma_f32_16x16x32_bf16 v[196:199], a[112:115], a[32:35], v[148:151]\n\t"
    "v_accvgpr_write_b32 a38, v172\n\t"
    "v_accvgpr_write_b32 a39, v174\n\t"
    "s_mov_b32 s60, 0x0\n\t"
    "s_add_u32 m0, s73, 0x0\n\t"
    "s_nop 0\n\t"
    "buffer_load_dwordx4 v226, s[28:31], s60 offen offset:2048 nt lds\n\t"
    "buffer_load_dwordx4 v227, s[28:31], s60 offen offset:3072 nt lds\n\t"
    "v_mfma_f32_16x16x32_bf16 v[168:171], a[230:233], a[44:47], 0\n\t"
    "v_cvt_pk_bf16_f32 v128, v128, v129\n\t"
    "v_mfma_f32_16x16x32_bf16 v[172:175], a[228:231], a[44:47], 0\n\t"
    "v_cvt_pk_bf16_f32 v130, v130, v131\n\t"
    "s_add_u32 m0, s74, 0x1e000\n\t"
    "s_nop 0\n\t"
    "buffer_load_dwordx4 v190, s[40:43], 0 offen lds\n\t"
    "v_mfma_f32_16x16x32_bf16 v[168:171], a[234:237], a[44:47], v[168:171]\n\t"
    "v_cvt_pk_bf16_f32 v132, v132, v133\n\t"
    "buffer_load_dwordx4 v191, s[40:43], 0 offen offset:1024 lds\n\t"
    "v_mfma_f32_16x16x32_bf16 v[172:175], a[232:235], a[44:47], v[172:175]\n\t"
    "v_cvt_pk_bf16_f32 v134, v134, v135\n\t"
    "v_mfma_f32_16x16x32_bf16 v[192:195], a[100:103], a[36:39], v[192:195]\n\t"
    "v_accvgpr_write_b32 a40, v128\n\t"
    "v_accvgpr_write_b32 a41, v130\n\t"
    "s_xor_b32 s64, s67, 1\n\t"
    "v_add_u32 v230, s64, v230\n\t"
    "s_mov_b64 exec, 0xff\n\t"
    "buffer_store_dword v230, v176, s[56:59], 0 offen offset:128 sc1\n\t"
    "s_mov_b64 exec, -1\n\t"
    "v_mfma_f32_16x16x32_bf16 v[196:199], a[116:119], a[36:39], v[196:199]\n\t"
    "v_accvgpr_write_b32 a42, v132\n\t"
    "v_accvgpr_write_b32 a43, v134\n\t"
    "s_waitcnt lgkmcnt(0)\n\t"
    "v_mfma_f32_16x16x32_bf16 v[128:131], a[230:233], a[48:51], 0\n\t"
    "v_cvt_pk_bf16_f32 v168, v168, v169\n\t"
    "v_mfma_f32_16x16x32_bf16 v[132:135], a[228:231], a[48:51], 0\n\t"
    "v_cvt_pk_bf16_f32 v170, v170, v171\n\t"
    "s_mov_b32 s60, 0x40000000\n\t"
    "s_add_u32 m0, s74, 0x1c000\n\t"
    "s_nop 0\n\t"
    "buffer_load_dwordx4 v190, s[40:43], s60 offen lds\n\t"
    "v_mfma_f32_16x16x32_bf16 v[128:131], a[234:237], a[48:51], v[128:131]\n\t"
    "v_cvt_pk_bf16_f32 v172, v172, v173\n\t"
    "v_mfma_f32_16x16x32_bf16 v[132:135], a[232:235], a[48:51], v[132:135]\n\t"
    "v_cvt_pk_bf16_f32 v174, v174, v175\n\t"
    "buffer_load_dwordx4 v190, s[40:43], s60 offen offset:1024 lds\n\t"
    "v_mfma_f32_16x16x32_bf16 v[192:195], a[104:107], a[40:43], v[192:195]\n\t"
    "v_accvgpr_write_b32 a44, v168\n\t"
    "v_accvgpr_write_b32 a45, v170\n\t"
    "v_mfma_f32_16x16x32_bf16 v[196:199], a[120:123], a[40:43], v[196:199]\n\t"
    "v_accvgpr_write_b32 a46, v172\n\t"
    "v_accvgpr_write_b32 a47, v174\n\t"
    "v_mfma_f32_16x16x32_bf16 v[168:171], a[230:233], a[52:55], 0\n\t"
    "v_cvt_pk_bf16_f32 v128, v128, v129\n\t"
    "v_mfma_f32_16x16x32_bf16 v[172:175], a[228:231], a[52:55], 0\n\t"
    "v_cvt_pk_bf16_f32 v130, v130, v131\n\t"
    "s_waitcnt vmcnt(11)\n\t"
    "v_mfma_f32_16x16x32_bf16 v[168:171], a[234:237], a[52:55], v[168:171]\n\t"
    "v_cvt_pk_bf16_f32 v132, v132, v133\n\t"
    "v_mfma_f32_16x16x32_bf16 v[172:175], a[232:235], a[52:55], v[172:175]\n\t"
    "v_cvt_pk_bf16_f32 v134, v134, v135\n\t"
    "ds_read_b128 a[0:3], v136 offset:32768\n\t"
    "v_mfma_f32_16x16x32_bf16 v[192:195], a[108:111], a[44:47], v[192:195]\n\t"
    "v_accvgpr_write_b32 a48, v128\n\t"
    "v_accvgpr_write_b32 a49, v130\n\t"
    "ds_read_b128 a[4:7], v137 offset:32768\n\t"
    "v_mfma_f32_16x16x32_bf16 v[196:199], a[124:127], a[44:47], v[196:199]\n\t"
    "v_accvgpr_write_b32 a50, v132\n\t"
    "v_accvgpr_write_b32 a51, v134\n\t"
    "ds_read_b128 a[8:11], v138 offset:32768\n\t"
    "ds_read_b128 a[12:15], v139 offset:32768\n\t"
    "v_mfma_f32_16x16x32_bf16 v[128:131], a[230:233], a[56:59], 0\n\t"
    "v_cvt_pk_bf16_f32 v168, v168, v169\n\t"
    "v_mfma_f32_16x16x32_bf16 v[132:135], a[228:231], a[56:59], 0\n\t"
    "v_cvt_pk_bf16_f32 v170, v170, v171\n\t"
    "v_mfma_f32_16x16x32_bf16 v[128:131], a[234:237], a[56:59], v[128:131]\n\t"
    "v_cvt_pk_bf16_f32 v172, v172, v173\n\t"
    "v_mfma_f32_16x16x32_bf16 v[132:135], a[232:235], a[56:59], v[132:135]\n\t"
    "v_cvt_pk_bf16_f32 v174, v174, v175\n\t"
    "v_mfma_f32_16x16x32_bf16 v[200:203], a[96:99], a[48:51], v[152:155]\n\t"
    "v_accvgpr_write_b32 a52, v168\n\t"
    "v_accvgpr_write_b32 a53, v170\n\t"
    "v_mfma_f32_16x16x32_bf16 v[204:207], a[112:115], a[48:51], v[156:159]\n\t"
    "v_accvgpr_write_b32 a54, v172\n\t"
    "v_accvgpr_write_b32 a55, v174\n\t"
    "s_mov_b32 s60, 0x4000\n\t"
    "s_add_u32 m0, s73, 0x4000\n\t"
    "s_nop 0\n\t"
    "buffer_load_dwordx4 v224, s[28:31], s60 offen nt lds\n\t"
    "v_mfma_f32_16x16x32_bf16 v[168:171], a[230:233], a[60:63], 0\n\t"
    "v_cvt_pk_bf16_f32 v128, v128, v129\n\t"
    "v_mfma_f32_16x16x32_bf16 v[172:175], a[228:231], a[60:63], 0\n\t"
    "v_cvt_pk_bf16_f32 v130, v130, v131\n\t"
    "v_mfma_f32_16x16x32_bf16 v[168:171], a[234:237], a[60:63], v[168:171]\n\t"
    "v_cvt_pk_bf16_f32 v132, v132, v133\n\t"
    "buffer_load_dwordx4 v225, s[28:31], s60 offen offset:1024 nt lds\n\t"
    "v_mfma_f32_16x16x32_bf16 v[172:175], a[232:235], a[60:63], v[172:175]\n\t"
    "v_cvt_pk_bf16_f32 v134, v134, v135\n\t"
    "v_mfma_f32_16x16x32_bf16 v[200:203], a[100:103], a[52:55], v[200:203]\n\t"
    "v_accvgpr_write_b32 a56, v128\n\t"
    "v_accvgpr_write_b32 a57, v130\n\t"
    "v_mfma_f32_16x16x32_bf16 v[204:207], a[116:119], a[52:55], v[204:207]\n\t"
    "v_accvgpr_write_b32 a58, v132\n\t"
    "v_accvgpr_write_b32 a59, v134\n\t"
    "s_waitcnt lgkmcnt(0)\n\t"
    "v_mfma_f32_16x16x32_bf16 v[128:131], a[230:233], a[0:3], 0\n\t"
    "v_cvt_pk_bf16_f32 v168, v168, v169\n\t"
    "v_mfma_f32_16x16x32_bf16 v[132:135], a[228:231], a[0:3], 0\n\t"
    "v_cvt_pk_bf16_f32 v170, v170, v171\n\t"
    "buffer_load_dwordx4 v226, s[28:31], s60 offen offset:2048 nt lds\n\t"
    "v_mfma_f32_16x16x32_bf16 v[128:131], a[234:237], a[0:3], v[128:131]\n\t"
    "v_cvt_pk_bf16_f32 v172, v172, v173\n\t"
    "buffer_load_dwordx4 v227, s[28:31], s60 offen offset:3072 nt lds\n\t"
    "v_mfma_f32_16x16x32_bf16 v[132:135], a[232:235], a[0:3], v[132:135]\n\t"
    "v_cvt_pk_bf16_f32 v174, v174, v175\n\t"
    "v_max3_f32 v216, v192, v193, v194\n\t"
    "v_mfma_f32_16x16x32_bf16 v[200:203], a[104:107], a[56:59], v[200:203]\n\t"
    "v_accvgpr_write_b32 a60, v168\n\t"
    "v_accvgpr_write_b32 a61, v170\n\t"
    "v_max3_f32 v216, v216, v195, v196\n\t"
    "v_mfma_f32_16x16x32_bf16 v[204:207], a[120:123], a[56:59], v[204:207]\n\t"
    "v_accvgpr_write_b32 a62, v172\n\t"
    "v_accvgpr_write_b32 a63, v174\n\t"
    "v_max3_f32 v216, v216, v197, v198\n\t"
    "v_mfma_f32_16x16x32_bf16 v[168:171], a[230:233], a[4:7], 0\n\t"
    "v_cvt_pk_bf16_f32 v128, v128, v129\n\t"
    "v_max_f32 v216, v216, v199\n\t"
    "v_mfma_f32_16x16x32_bf16 v[172:175], a[228:231], a[4:7], 0\n\t"
    "v_cvt_pk_bf16_f32 v130, v130, v131\n\t"
    "v_mov_b32 v251, v216\n\t"
    "s_nop 1\n\t"
    "v_permlane32_swap_b32 v216, v251\n\t"
    "v_mfma_f32_16x16x32_bf16 v[168:171], a[234:237], a[4:7], v[168:171]\n\t"
    "v_cvt_pk_bf16_f32 v132, v132, v133\n\t"
    "v_mfma_f32_16x16x32_bf16 v[172:175], a[232:235], a[4:7], v[172:175]\n\t"
    "v_cvt_pk_bf16_f32 v134, v134, v135\n\t"
    "s_waitcnt vmcnt(11)\n\t"
    "ds_read_b128 a[16:19], v136 offset:49152\n\t"
    "v_mfma_f32_16x16x32_bf16 v[200:203], a[108:111], a[60:63], v[200:203]\n\t"
    "v_accvgpr_write_b32 a0, v128\n\t"
    "v_accvgpr_write_b32 a1, v130\n\t"
    "ds_read_b128 a[20:23], v137 offset:49152\n\t"
    "v_mfma_f32_16x16x32_bf16 v[204:207], a[124:127], a[60:63], v[204:207]\n\t"
    "v_accvgpr_write_b32 a2, v132\n\t"
    "v_accvgpr_write_b32 a3, v134\n\t"
    "ds_read_b128 a[24:27], v138 offset:49152\n\t"
    "ds_read_b128 a[28:31], v139 offset:49152\n\t"
    "v_mfma_f32_16x16x32_bf16 v[128:131], a[230:233], a[8:11], 0\n\t"
    "v_cvt_pk_bf16_f32 v168, v168, v169\n\t"
    "v_mfma_f32_16x16x32_bf16 v[132:135], a[228:231], a[8:11], 0\n\t"
    "v_cvt_pk_bf16_f32 v170, v170, v171\n\t"
    "v_max_f32 v216, v216, v251\n\t"
    "v_mfma_f32_16x16x32_bf16 v[128:131], a[234:237], a[8:11], v[128:131]\n\t"
    "v_cvt_pk_bf16_f32 v172, v172, v173\n\t"
    "v_mov_b32 v251, v216\n\t"
    "v_mfma_f32_16x16x32_bf16 v[132:135], a[232:235], a[8:11], v[132:135]\n\t"
    "v_cvt_pk_bf16_f32 v174, v174, v175\n\t"
    "v_permlane16_swap_b32 v216, v251\n\t"
    "v_mfma_f32_16x16x32_bf16 v[160:163], a[96:99], a[0:3], v[144:147]\n\t"
    "v_accvgpr_write_b32 a4, v168\n\t"
    "v_accvgpr_write_b32 a5, v170\n\t"
    "v_max_f32 v216, v216, v251\n\t"
    "v_mfma_f32_16x16x32_bf16 v[164:167], a[112:115], a[0:3], v[148:151]\n\t"
    "v_accvgpr_write_b32 a6, v172\n\t"
    "v_accvgpr_write_b32 a7, v174\n\t"
    "s_mov_b32 s60, 0x0\n\t"
    "s_add_u32 m0, s73, 0x10000\n\t"
    "s_nop 0\n\t"
    "buffer_load_dwordx4 v228, s[32:35], s60 offen nt lds\n\t"
    "v_mfma_f32_16x16x32_bf16 v[168:171], a[230:233], a[12:15], 0\n\t"
    "v_cvt_pk_bf16_f32 v128, v128, v129\n\t"
    "v_mfma_f32_16x16x32_bf16 v[172:175], a[228:231], a[12:15], 0\n\t"
    "v_cvt_pk_bf16_f32 v130, v130, v131\n\t"
    "v_mfma_f32_16x16x32_bf16 v[168:171], a[234:237], a[12:15], v[168:171]\n\t"
    "v_cvt_pk_bf16_f32 v132, v132, v133\n\t"
    "buffer_load_dwordx4 v229, s[32:35], s60 offen offset:1024 nt lds\n\t"
    "v_mfma_f32_16x16x32_bf16 v[172:175], a[232:235], a[12:15], v[172:175]\n\t"
    "v_cvt_pk_bf16_f32 v134, v134, v135\n\t"
    "v_mfma_f32_16x16x32_bf16 v[160:163], a[100:103], a[4:7], v[160:163]\n\t"
    "v_accvgpr_write_b32 a8, v128\n\t"
    "v_accvgpr_write_b32 a9, v130\n\t"
    "v_mfma_f32_16x16x32_bf16 v[164:167], a[116:119], a[4:7], v[164:167]\n\t"
    "v_accvgpr_write_b32 a10, v132\n\t"
    "v_accvgpr_write_b32 a11, v134\n\t"
    "s_waitcnt lgkmcnt(0)\n\t"
    "v_mfma_f32_16x16x32_bf16 v[128:131], a[230:233], a[16:19], 0\n\t"
    "v_cvt_pk_bf16_f32 v168, v168, v169\n\t"
    "v_mfma_f32_16x16x32_bf16 v[132:135], a[228:231], a[16:19], 0\n\t"
    "v_cvt_pk_bf16_f32 v170, v170, v171\n\t"
    "buffer_load_dwordx4 v228, s[32:35], s60 offen offset:2048 nt lds\n\t"
    "v_mfma_f32_16x16x32_bf16 v[128:131], a[234:237], a[16:19], v[128:131]\n\t"
    "v_cvt_pk_bf16_f32 v172, v172, v173\n\t"
    "v_max3_f32 v220, v200, v201, v202\n\t"
    "v_mfma_f32_16x16x32_bf16 v[132:135], a[232:235], a[16:19], v[132:135]\n\t"
    "v_cvt_pk_bf16_f32 v174, v174, v175\n\t"
    "v_mfma_f32_16x16x32_bf16 v[160:163], a[104:107], a[8:11], v[160:163]\n\t"
    "v_accvgpr_write_b32 a12, v168\n\t"
    "v_accvgpr_write_b32 a13, v170\n\t"
    "v_max3_f32 v220, v220, v203, v204\n\t"
    "v_mfma_f32_16x16x32_bf16 v[164:167], a[120:123], a[8:11], v[164:167]\n\t"
    "v_accvgpr_write_b32 a14, v172\n\t"
    "v_accvgpr_write_b32 a15, v174\n\t"
    "v_max3_f32 v220, v220, v205, v206\n\t"
    "v_mfma_f32_16x16x32_bf16 v[168:171], a[230:233], a[20:23], 0\n\t"
    "v_cvt_pk_bf16_f32 v128, v128, v129\n\t"
    "v_mfma_f32_16x16x32_bf16 v[172:175], a[228:231], a[20:23], 0\n\t"
    "v_cvt_pk_bf16_f32 v130, v130, v131\n\t"
    "buffer_load_dwordx4 v229, s[32:35], s60 offen offset:3072 nt lds\n\t"
    "v_mfma_f32_16x16x32_bf16 v[168:171], a[234:237], a[20:23], v[168:171]\n\t"
    "v_cvt_pk_bf16_f32 v132, v132, v133\n\t"
    "v_max_f32 v220, v220, v207\n\t"
    "v_mfma_f32_16x16x32_bf16 v[172:175], a[232:235], a[20:23], v[172:175]\n\t"
    "v_cvt_pk_bf16_f32 v134, v134, v135\n\t"
    "v_mfma_f32_16x16x32_bf16 v[160:163], a[108:111], a[12:15], v[160:163]\n\t"
    "v_accvgpr_write_b32 a16, v128\n\t"
    "v_accvgpr_write_b32 a17, v130\n\t"
    "v_mov_b32 v251, v220\n\t"
    "v_mfma_f32_16x16x32_bf16 v[164:167], a[124:127], a[12:15], v[164:167]\n\t"
    "v_accvgpr_write_b32 a18, v132\n\t"
    "v_accvgpr_write_b32 a19, v134\n\t"
    "v_permlane32_swap_b32 v220, v251\n\t"
    "v_mfma_f32_16x16x32_bf16 v[128:131], a[230:233], a[24:27], 0\n\t"
    "v_cvt_pk_bf16_f32 v168, v168, v169\n\t"
    "v_max_f32 v220, v220, v251\n\t"
    "v_mfma_f32_16x16x32_bf16 v[132:135], a[228:231], a[24:27], 0\n\t"
    "v_cvt_pk_bf16_f32 v170, v170, v171\n\t"
    "v_mov_b32 v251, v220\n\t"
    "v_mfma_f32_16x16x32_bf16 v[128:131], a[234:237], a[24:27], v[128:131]\n\t"
    "v_cvt_pk_bf16_f32 v172, v172, v173\n\t"
    "v_permlane16_swap_b32 v220, v251\n\t"
    "v_max_f32 v220, v220, v251\n\t"
    "v_mfma_f32_16x16x32_bf16 v[132:135], a[232:235], a[24:27], v[132:135]\n\t"
    "v_cvt_pk_bf16_f32 v174, v174, v175\n\t"
    "v_mfma_f32_16x16x32_bf16 v[176:179], a[96:99], a[16:19], v[152:155]\n\t"
    "v_accvgpr_write_b32 a20, v168\n\t"
    "v_accvgpr_write_b32 a21, v170\n\t"
    "s_waitcnt vmcnt(8)\n\t"
    "v_mfma_f32_16x16x32_bf16 v[180:183], a[112:115], a[16:19], v[156:159]\n\t"
    "v_accvgpr_write_b32 a22, v172\n\t"
    "v_accvgpr_write_b32 a23, v174\n\t"
    "s_barrier\n\t"
    "v_mfma_f32_16x16x32_bf16 v[168:171], a[230:233], a[28:31], 0\n\t"
    "v_cvt_pk_bf16_f32 v128, v128, v129\n\t"
    "ds_read_b128 a[64:67], v232 offset:0\n\t"
    "v_mfma_f32_16x16x32_bf16 v[172:175], a[228:231], a[28:31], 0\n\t"
    "v_cvt_pk_bf16_f32 v130, v130, v131\n\t"
    "ds_read_b128 a[68:71], v233 offset:0\n\t"
    "ds_read_b128 a[72:75], v234 offset:0\n\t"
    "v_mfma_f32_16x16x32_bf16 v[168:171], a[234:237], a[28:31], v[168:171]\n\t"
    "v_cvt_pk_bf16_f32 v132, v132, v133\n\t"
    "ds_read_b128 a[76:79], v235 offset:0\n\t"
    "ds_read_b128 a[80:83], v232 offset:4096\n\t"
    "v_mfma_f32_16x16x32_bf16 v[172:175], a[232:235], a[28:31], v[172:175]\n\t"
    "v_cvt_pk_bf16_f32 v134, v134, v135\n\t"
    "s_mov_b32 s60, 0x8000\n\t"
    "s_add_u32 m0, s73, 0x8000\n\t"
    "s_nop 0\n\t"
    "buffer_load_dwordx4 v224, s[28:31], s60 offen nt lds\n\t"
    "v_mfma_f32_16x16x32_bf16 v[176:179], a[100:103], a[20:23], v[176:179]\n\t"
    "v_accvgpr_write_b32 a24, v128\n\t"
    "v_accvgpr_write_b32 a25, v130\n\t"
    "v_max3_f32 v208, v160, v161, v162\n\t"
    "v_max3_f32 v208, v208, v163, v164\n\t"
    "v_mfma_f32_16x16x32_bf16 v[180:183], a[116:119], a[20:23], v[180:183]\n\t"
    "v_accvgpr_write_b32 a26, v132\n\t"
    "v_accvgpr_write_b32 a27, v134\n\t"
    "v_max3_f32 v208, v208, v165, v166\n\t"
    "v_min_f32 v249, v216, v220\n\t"
    "v_mfma_f32_16x16x32_bf16 v[176:179], a[104:107], a[24:27], v[176:179]\n\t"
    "v_cvt_pk_bf16_f32 v168, v168, v169\n\t"
    "ds_read_b128 a[84:87], v233 offset:4096\n\t"
    "v_mfma_f32_16x16x32_bf16 v[180:183], a[120:123], a[24:27], v[180:183]\n\t"
    "v_cvt_pk_bf16_f32 v170, v170, v171\n\t"
    "ds_read_b128 a[88:91], v234 offset:4096\n\t"
    "ds_read_b128 a[92:95], v235 offset:4096\n\t"
    "v_cvt_pk_bf16_f32 v172, v172, v173\n\t"
    "ds_read_b64_tr_b16 a[160:161], v236 offset:57344\n\t"
    "ds_read_b64_tr_b16 a[162:163], v236 offset:61440\n\t"
    "v_cvt_pk_bf16_f32 v174, v174, v175\n\t"
    "buffer_load_dwordx4 v225, s[28:31], s60 offen offset:1024 nt lds\n\t"
    "v_accvgpr_write_b32 a28, v168\n\t"
    "v_accvgpr_write_b32 a29, v170\n\t"
    "v_max_f32 v208, v208, v167\n\t"
    "v_mov_b32 v251, v208\n\t"
    "v_accvgpr_write_b32 a30, v172\n\t"
    "v_accvgpr_write_b32 a31, v174\n\t"
    "v_permlane32_swap_b32 v208, v251\n\t"
    "v_cmp_gt_f32 vcc, 0xc9742400, v249\n\t"
    "v_mfma_f32_16x16x32_bf16 v[176:179], a[108:111], a[28:31], v[176:179]\n\t"
    "ds_read_b64_tr_b16 a[164:165], v237 offset:57344\n\t"
    "ds_read_b64_tr_b16 a[166:167], v237 offset:61440\n\t"
    "ds_read_b64_tr_b16 a[168:169], v238 offset:57344\n\t"
    "ds_read_b64_tr_b16 a[170:171], v238 offset:61440\n\t"
    "ds_read_b64_tr_b16 a[172:173], v239 offset:57344\n\t"
    "ds_read_b64_tr_b16 a[174:175], v239 offset:61440\n\t"
    "v_mfma_f32_16x16x32_bf16 v[180:183], a[124:127], a[28:31], v[180:183]\n\t"
    "buffer_load_dwordx4 v226, s[28:31], s60 offen offset:2048 nt lds\n\t"
    "buffer_load_dwordx4 v227, s[28:31], s60 offen offset:3072 nt lds\n\t"
    "v_max_f32 v208, v208, v251\n\t"
    "v_mov_b32 v251, v208\n\t"
    "s_nop 1\n\t"
    "v_permlane16_swap_b32 v208, v251\n\t"
    "v_max_f32 v208, v208, v251\n\t"
    "s_cbranch_vccnz .Lfix%=\n\t"
    ".Lnofix%=:\n\t"
    "v_mfma_f32_32x32x16_bf16 v[160:175], a[236:239], a[236:239], 0\n\t"
    "v_add_f32 v249, s75, v216\n\t"
    "v_add_f32 v250, s75, v220\n\t"
    "v_mul_f32 v249, s53, v249\n\t"
    "v_fma_f32 v192, v192, s53, -v249\n\t"
    "v_max3_f32 v251, v176, v177, v178\n\t"
    "v_fma_f32 v193, v193, s53, -v249\n\t"
    "v_fma_f32 v194, v194, s53, -v249\n\t"
    "v_fma_f32 v195, v195, s53, -v249\n\t"
    "v_max3_f32 v251, v251, v179, v180\n\t"
    "v_fma_f32 v196, v196, s53, -v249\n\t"
    "v_fma_f32 v197, v197, s53, -v249\n\t"
    "v_fma_f32 v198, v198, s53, -v249\n\t"
    "v_max3_f32 v251, v251, v181, v182\n\t"
    "v_fma_f32 v199, v199, s53, -v249\n\t"
    "v_sub_f32 v200, v200, v250\n\t"
    "v_sub_f32 v201, v201, v250\n\t"
    "v_max_f32 v251, v251, v183\n\t"
    "v_sub_f32 v202, v202, v250\n\t"
    "v_sub_f32 v203, v203, v250\n\t"
    "v_sub_f32 v204, v204, v250\n\t"
    "v_mov_b32 v248, v251\n\t"
    "v_sub_f32 v205, v205, v250\n\t"
    "v_sub_f32 v206, v206, v250\n\t"
    "v_sub_f32 v207, v207, v250\n\t"
    "v_exp_f32 v192, v192\n\t"
    "v_permlane32_swap_b32 v251, v248\n\t"
    "v_exp_f32 v193, v193\n\t"
    "v_exp_f32 v194, v194\n\t"
    "v_exp_f32 v195, v195\n\t"
    "v_max_f32 v251, v251, v248\n\t"
    "v_exp_f32 v196, v196\n\t"
    "v_exp_f32 v197, v197\n\t"
    "v_exp_f32 v198, v198\n\t"
    "v_mov_b32 v248, v251\n\t"
    "v_exp_f32 v199, v199\n\t"
    "v_sub_f32 v208, 0, v208\n\t"
    "v_mov_b32 v209, v208\n\t"
    "v_permlane16_swap_b32 v251, v248\n\t"
    "v_mov_b32 v210, v208\n\t"
    "v_mov_b32 v211, v208\n\t"
    "v_sub_f32 v216, 0, v216\n\t"
    "v_max_f32 v251, v251, v248\n\t"
    "v_mov_b32 v217, v216\n\t"
    "v_mov_b32 v218, v216\n\t"
    "v_mov_b32 v219, v216\n\t"
    "v_max_f32 v212, v212, v251\n\t"
    "v_cvt_pk_bf16_f32 v184, v192, v193\n\t"
    "v_cvt_pk_bf16_f32 v185, v194, v195\n\t"
    "v_cvt_pk_bf16_f32 v186, v196, v197\n\t"
    "v_cvt_pk_bf16_f32 v187, v198, v199\n\t"
    "v_sub_f32 v220, 0, v220\n\t"
    "v_mov_b32 v221, v220\n\t"
    "v_mov_b32 v222, v220\n\t"
    "v_mov_b32 v223, v220\n\t"
    "v_sub_f32 v212, 0, v212\n\t"
    "v_mov_b32 v213, v212\n\t"
    "v_mov_b32 v214, v212\n\t"
    "v_mov_b32 v215, v212\n\t"
    "v_mov_b32 v176, 0\n\t"
    "v_mov_b32 v177, 0\n\t"
    "v_mov_b32 v178, 0\n\t"
    "v_mov_b32 v179, 0\n\t"
    "v_mov_b32 v180, 0\n\t"
    "v_mov_b32 v181, 0\n\t"
    "v_mov_b32 v182, 0\n\t"
    "v_mov_b32 v183, 0\n\t"
    "buffer_load_dword v254, off, s[48:51], 0\n\t"
    "s_mov_b32 s60, 0x4000\n\t"
    "s_mov_b32 s62, 0xc000\n\t"
    "s_waitcnt lgkmcnt(0)\n\t"
    "s_cmp_lg_u32 s67, 0\n\t"
    "s_cbranch_scc1 .Lsloop%=\n\t"
    "s_lshr_b32 s97, s71, 3\n\t"
    "s_mul_i32 s97, s97, 25\n\t"
    "s_add_u32 s96, s96, s97\n\t"
    "s_add_u32 s96, s96, 87\n\t"
    "s_mov_b32 s97, 146\n\t"
    "s_branch .Lloop%=\n\t"
    ".Lpad1_%=:\n\t"
    "s_cmp_gt_u32 s60, 0x38000\n\t"
    "s_cbranch_scc1 .Lpadb1_%=\n\t"
    "s_memrealtime s[98:99]\n\t"
    "s_waitcnt lgkmcnt(0)\n\t"
    "s_sub_u32 s98, s98, s96\n\t"
    "s_cmp_lt_i32 s98, 62\n\t"
    "s_cbranch_scc1 .Lpad1_%=\n\t"
    "s_branch .Lpadb1_%=\n\t"
    ".Lpad3_%=:\n\t"
    "s_cmp_gt_u32 s60, 0x38000\n\t"
    "s_cbranch_scc1 .Lpadb3_%=\n\t"
    "s_memrealtime s[98:99]\n\t"
    "s_waitcnt lgkmcnt(0)\n\t"
    "s_sub_u32 s98, s98, s96\n\t"
    "s_cmp_lt_i32 s98, 62\n\t"
    "s_cbranch_scc1 .Lpad3_%=\n\t"
    "s_branch .Lpadb3_%=\n\t"
    ".Lpad5_%=:\n\t"
    "s_cmp_gt_u32 s60, 0x38000\n\t"
    "s_cbranch_scc1 .Lpadb5_%=\n\t"
    "s_memrealtime s[98:99]\n\t"
    "s_waitcnt lgkmcnt(0)\n\t"
    "s_sub_u32 s98, s98, s96\n\t"
    "s_cmp_lt_i32 s98, 62\n\t"
    "s_cbranch_scc1 .Lpad5_%=\n\t"
    "s_branch .Lpadb5_%=\n\t"
    ".Lpad7_%=:\n\t"
    "s_cmp_gt_u32 s60, 0x38000\n\t"
    "s_cbranch_scc1 .Lpadb7_%=\n\t"
    "s_memrealtime s[98:99]\n\t"
    "s_waitcnt lgkmcnt(0)\n\t"
    "s_sub_u32 s98, s98, s96\n\t"
    "s_cmp_lt_i32 s98, 62\n\t"
    "s_cbranch_scc1 .Lpad7_%=\n\t"
    "s_branch .Lpadb7_%=\n\t"
    ".Lloop%=:\n\t"
    "v_mfma_f32_16x16x32_bf16 v[192:195], a[64:67], a[0:3], v[208:211]\n\t"
    "v_exp_f32 v200, v200\n\t"
    "v_mfma_f32_16x16x32_bf16 v[192:195], a[68:71], a[4:7], v[192:195]\n\t"
    "v_exp_f32 v201, v201\n\t"
    "v_mfma_f32_16x16x32_bf16 v[192:195], a[72:75], a[8:11], v[192:195]\n\t"
    "v_cvt_pk_bf16_f32 v188, v200, v201\n\t"
    "v_mfma_f32_16x16x32_bf16 v[192:195], a[76:79], a[12:15], v[192:195]\n\t"
    "ds_read_b64_tr_b16 a[144:145], v240 offset:49152\n\t"
    "ds_read_b64_tr_b16 a[146:147], v240 offset:53248\n\t"
    "v_mfma_f32_16x16x32_bf16 v[196:199], a[80:83], a[0:3], v[208:211]\n\t"
    "v_exp_f32 v202, v202\n\t"
    "v_mfma_f32_16x16x32_bf16 v[196:199], a[84:87], a[4:7], v[196:199]\n\t"
    "v_exp_f32 v203, v203\n\t"
    "v_mfma_f32_16x16x32_bf16 v[196:199], a[88:91], a[8:11], v[196:199]\n\t"
    "v_cvt_pk_bf16_f32 v189, v202, v203\n\t"
    "v_mfma_f32_16x16x32_bf16 v[196:199], a[92:95], a[12:15], v[196:199]\n\t"
    "s_add_u32 m0, s73, 0x14000\n\t"
    "v_mfma_f32_16x16x32_bf16 a[192:195], a[208:211], v[160:163], a[192:195]\n\t"
    "v_exp_f32 v204, v204\n\t"
    "v_mfma_f32_16x16x32_bf16 v[0:3], a[128:131], v[160:163], v[0:3]\n\t"
    "v_exp_f32 v205, v205\n\t"
    "v_mfma_f32_16x16x32_bf16 v[0:3], a[160:163], v[176:179], v[0:3]\n\t"
    "v_cvt_pk_bf16_f32 v190, v204, v205\n\t"
    "v_mfma_f32_16x16x32_bf16 v[32:35], a[128:131], v[164:167], v[32:35]\n\t"
    "ds_read_b64_tr_b16 a[176:177], v240 offset:57344\n\t"
    "ds_read_b64_tr_b16 a[178:179], v240 offset:61440\n\t"
    "v_mfma_f32_16x16x32_bf16 v[32:35], a[160:163], v[180:183], v[32:35]\n\t"
    "v_exp_f32 v206, v206\n\t"
    "v_mfma_f32_16x16x32_bf16 v[64:67], a[128:131], v[168:171], v[64:67]\n\t"
    "v_exp_f32 v207, v207\n\t"
    "v_mfma_f32_16x16x32_bf16 v[64:67], a[160:163], v[184:187], v[64:67]\n\t"
    "v_cvt_pk_bf16_f32 v191, v206, v207\n\t"
    "v_mfma_f32_16x16x32_bf16 v[96:99], a[128:131], v[172:175], v[96:99]\n\t"
    "buffer_load_dwordx4 v228, s[32:35], s60 offen nt lds\n\t"
    "v_mfma_f32_16x16x32_bf16 v[96:99], a[160:163], v[188:191], v[96:99]\n\t"
    "ds_read_b128 a[96:99], v232 offset:8192\n\t"
    "ds_read_b128 a[100:103], v233 offset:8192\n\t"
    "v_mfma_f32_16x16x32_bf16 v[200:203], a[64:67], a[16:19], v[212:215]\n\t"
    "v_exp_f32 v192, v192\n\t"
    "v_mfma_f32_16x16x32_bf16 v[200:203], a[68:71], a[20:23], v[200:203]\n\t"
    "v_exp_f32 v193, v193\n\t"
    "v_mfma_f32_16x16x32_bf16 v[200:203], a[72:75], a[24:27], v[200:203]\n\t"
    "v_cvt_pk_bf16_f32 v128, v192, v193\n\t"
    "v_mfma_f32_16x16x32_bf16 v[200:203], a[76:79], a[28:31], v[200:203]\n\t"
    "ds_read_b64_tr_b16 a[148:149], v241 offset:49152\n\t"
    "ds_read_b64_tr_b16 a[150:151], v241 offset:53248\n\t"
    "v_mfma_f32_16x16x32_bf16 v[204:207], a[80:83], a[16:19], v[212:215]\n\t"
    "v_exp_f32 v194, v194\n\t"
    "v_mfma_f32_16x16x32_bf16 v[204:207], a[84:87], a[20:23], v[204:207]\n\t"
    "v_exp_f32 v195, v195\n\t"
    "v_mfma_f32_16x16x32_bf16 v[204:207], a[88:91], a[24:27], v[204:207]\n\t"
    "v_cvt_pk_bf16_f32 v129, v194, v195\n\t"
    "v_mfma_f32_16x16x32_bf16 v[204:207], a[92:95], a[28:31], v[204:207]\n\t"
    "buffer_load_dwordx4 v229, s[32:35], s60 offen offset:1024 nt lds\n\t"
    "v_mfma_f32_16x16x32_bf16 a[196:199], a[208:211], v[164:167], a[196:199]\n\t"
    "v_exp_f32 v196, v196\n\t"
    "v_mfma_f32_16x16x32_bf16 v[4:7], a[132:135], v[160:163], v[4:7]\n\t"
    "v_exp_f32 v197, v197\n\t"
    "v_mfma_f32_16x16x32_bf16 v[4:7], a[164:167], v[176:179], v[4:7]\n\t"
    "v_cvt_pk_bf16_f32 v130, v196, v197\n\t"
    "v_mfma_f32_16x16x32_bf16 v[36:39], a[132:135], v[164:167], v[36:39]\n\t"
    "ds_read_b64_tr_b16 a[180:181], v241 offset:57344\n\t"
    "ds_read_b64_tr_b16 a[182:183], v241 offset:61440\n\t"
    "v_mfma_f32_16x16x32_bf16 v[36:39], a[164:167], v[180:183], v[36:39]\n\t"
    "v_exp_f32 v198, v198\n\t"
    "v_mfma_f32_16x16x32_bf16 v[68:71], a[132:135], v[168:171], v[68:71]\n\t"
    "v_exp_f32 v199, v199\n\t"
    "v_mfma_f32_16x16x32_bf16 v[68:71], a[164:167], v[184:187], v[68:71]\n\t"
    "v_cvt_pk_bf16_f32 v131, v198, v199\n\t"
    "v_mfma_f32_16x16x32_bf16 v[100:103], a[132:135], v[172:175], v[100:103]\n\t"
    "s_memrealtime s[98:99]\n\t"
    "s_add_u32 s96, s96, s97\n\t"
    "v_mfma_f32_16x16x32_bf16 v[100:103], a[164:167], v[188:191], v[100:103]\n\t"
    "ds_read_b128 a[104:107], v234 offset:8192\n\t"
    "ds_read_b128 a[108:111], v235 offset:8192\n\t"
    "v_mfma_f32_16x16x32_bf16 v[192:195], a[64:67], a[32:35], v[216:219]\n\t"
    "v_exp_f32 v200, v200\n\t"
    "v_mfma_f32_16x16x32_bf16 v[192:195], a[68:71], a[36:39], v[192:195]\n\t"
    "v_exp_f32 v201, v201\n\t"
    "v_mfma_f32_16x16x32_bf16 v[192:195], a[72:75], a[40:43], v[192:195]\n\t"
    "v_cvt_pk_bf16_f32 v132, v200, v201\n\t"
    "v_mfma_f32_16x16x32_bf16 v[192:195], a[76:79], a[44:47], v[192:195]\n\t"
    "ds_read_b64_tr_b16 a[152:153], v242 offset:49152\n\t"
    "ds_read_b64_tr_b16 a[154:155], v242 offset:53248\n\t"
    "v_mfma_f32_16x16x32_bf16 v[196:199], a[80:83], a[32:35], v[216:219]\n\t"
    "v_exp_f32 v202, v202\n\t"
    "v_mfma_f32_16x16x32_bf16 v[196:199], a[84:87], a[36:39], v[196:199]\n\t"
    "v_exp_f32 v203, v203\n\t"
    "v_mfma_f32_16x16x32_bf16 v[196:199], a[88:91], a[40:43], v[196:199]\n\t"
    "v_cvt_pk_bf16_f32 v133, v202, v203\n\t"
    "v_mfma_f32_16x16x32_bf16 v[196:199], a[92:95], a[44:47], v[196:199]\n\t"
    "buffer_load_dwordx4 v228, s[32:35], s60 offen offset:2048 nt lds\n\t"
    "v_mfma_f32_16x16x32_bf16 a[200:203], a[208:211], v[168:171], a[200:203]\n\t"
    "v_exp_f32 v204, v204\n\t"
    "v_mfma_f32_16x16x32_bf16 v[8:11], a[136:139], v[160:163], v[8:11]\n\t"
    "v_exp_f32 v205, v205\n\t"
    "v_mfma_f32_16x16x32_bf16 v[8:11], a[168:171], v[176:179], v[8:11]\n\t"
    "v_cvt_pk_bf16_f32 v134, v204, v205\n\t"
    "v_mfma_f32_16x16x32_bf16 v[40:43], a[136:139], v[164:167], v[40:43]\n\t"
    "ds_read_b64_tr_b16 a[184:185], v242 offset:57344\n\t"
    "ds_read_b64_tr_b16 a[186:187], v242 offset:61440\n\t"
    "v_mfma_f32_16x16x32_bf16 v[40:43], a[168:171], v[180:183], v[40:43]\n\t"
    "v_exp_f32 v206, v206\n\t"
    "v_mfma_f32_16x16x32_bf16 v[72:75], a[136:139], v[168:171], v[72:75]\n\t"
    "v_exp_f32 v207, v207\n\t"
    "v_mfma_f32_16x16x32_bf16 v[72:75], a[168:171], v[184:187], v[72:75]\n\t"
    "v_cvt_pk_bf16_f32 v135, v206, v207\n\t"
    "v_mfma_f32_16x16x32_bf16 v[104:107], a[136:139], v[172:175], v[104:107]\n\t"
    "buffer_load_dwordx4 v229, s[32:35], s60 offen offset:3072 nt lds\n\t"
    "v_mfma_f32_16x16x32_bf16 v[104:107], a[168:171], v[188:191], v[104:107]\n\t"
    "ds_read_b128 a[112:115], v232 offset:12288\n\t"
    "ds_read_b128 a[116:119], v233 offset:12288\n\t"
    "v_mfma_f32_16x16x32_bf16 v[200:203], a[64:67], a[48:51], v[220:223]\n\t"
    "v_exp_f32 v192, v192\n\t"
    "v_mfma_f32_16x16x32_bf16 v[200:203], a[68:71], a[52:55], v[200:203]\n\t"
    "v_exp_f32 v193, v193\n\t"
    "v_mfma_f32_16x16x32_bf16 v[200:203], a[72:75], a[56:59], v[200:203]\n\t"
    "v_cvt_pk_bf16_f32 v136, v192, v193\n\t"
    "v_mfma_f32_16x16x32_bf16 v[200:203], a[76:79], a[60:63], v[200:203]\n\t"
    "ds_read_b64_tr_b16 a[156:157], v243 offset:49152\n\t"
    "ds_read_b64_tr_b16 a[158:159], v243 offset:53248\n\t"
    "v_mfma_f32_16x16x32_bf16 v[204:207], a[80:83], a[48:51], v[220:223]\n\t"
    "v_exp_f32 v194, v194\n\t"
    "v_mfma_f32_16x16x32_bf16 v[204:207], a[84:87], a[52:55], v[204:207]\n\t"
    "v_exp_f32 v195, v195\n\t"
    "v_mfma_f32_16x16x32_bf16 v[204:207], a[88:91], a[56:59], v[204:207]\n\t"
    "v_cvt_pk_bf16_f32 v137, v194, v195\n\t"
    "v_mfma_f32_16x16x32_bf16 v[204:207], a[92:95], a[60:63], v[204:207]\n\t"
    "ds_read_b64_tr_b16 a[188:189], v243 offset:57344\n\t"
    "ds_read_b64_tr_b16 a[190:191], v243 offset:61440\n\t"
    "v_mfma_f32_16x16x32_bf16 a[204:207], a[208:211], v[172:175], a[204:207]\n\t"
    "v_exp_f32 v196, v196\n\t"
    "v_mfma_f32_16x16x32_bf16 v[12:15], a[140:143], v[160:163], v[12:15]\n\t"
    "v_exp_f32 v197, v197\n\t"
    "v_mfma_f32_16x16x32_bf16 v[12:15], a[172:175], v[176:179], v[12:15]\n\t"
    "v_cvt_pk_bf16_f32 v138, v196, v197\n\t"
    "v_mfma_f32_16x16x32_bf16 v[44:47], a[140:143], v[164:167], v[44:47]\n\t"
    "ds_read_b128 a[120:123], v234 offset:12288\n\t"
    "ds_read_b128 a[124:127], v235 offset:12288\n\t"
    "v_mfma_f32_16x16x32_bf16 v[44:47], a[172:175], v[180:183], v[44:47]\n\t"
    "v_exp_f32 v198, v198\n\t"
    "v_mfma_f32_16x16x32_bf16 v[76:79], a[140:143], v[168:171], v[76:79]\n\t"
    "v_exp_f32 v199, v199\n\t"
    "v_mfma_f32_16x16x32_bf16 v[76:79], a[172:175], v[184:187], v[76:79]\n\t"
    "v_cvt_pk_bf16_f32 v139, v198, v199\n\t"
    "v_mfma_f32_16x16x32_bf16 v[108:111], a[140:143], v[172:175], v[108:111]\n\t"
    "s_add_u32 s60, s60, 0x4000\n\t"
    "v_mfma_f32_16x16x32_bf16 v[108:111], a[172:175], v[188:191], v[108:111]\n\t"
    "s_waitcnt vmcnt(8) lgkmcnt(0)\n\t"
    "s_barrier\n\t"
    "v_mfma_f32_16x16x32_bf16 v[192:195], a[96:99], a[0:3], v[208:211]\n\t"
    "v_exp_f32 v200, v200\n\t"
    "v_mfma_f32_16x16x32_bf16 v[192:195], a[100:103], a[4:7], v[192:195]\n\t"
    "v_exp_f32 v201, v201\n\t"
    "v_mfma_f32_16x16x32_bf16 v[192:195], a[104:107], a[8:11], v[192:195]\n\t"
    "v_cvt_pk_bf16_f32 v140, v200, v201\n\t"
    "v_mfma_f32_16x16x32_bf16 v[192:195], a[108:111], a[12:15], v[192:195]\n\t"
    "ds_read_b64_tr_b16 a[128:129], v236 offset:0\n\t"
    "ds_read_b64_tr_b16 a[130:131], v236 offset:4096\n\t"
    "v_mfma_f32_16x16x32_bf16 v[196:199], a[112:115], a[0:3], v[208:211]\n\t"
    "v_exp_f32 v202, v202\n\t"
    "v_mfma_f32_16x16x32_bf16 v[196:199], a[116:119], a[4:7], v[196:199]\n\t"
    "v_exp_f32 v203, v203\n\t"
    "v_mfma_f32_16x16x32_bf16 v[196:199], a[120:123], a[8:11], v[196:199]\n\t"
    "v_cvt_pk_bf16_f32 v141, v202, v203\n\t"
    "v_mfma_f32_16x16x32_bf16 v[196:199], a[124:127], a[12:15], v[196:199]\n\t"
    "s_add_u32 m0, s73, 0xc000\n\t"
    "s_cmp_lt_u32 s98, s96\n\t"
    "v_mfma_f32_16x16x32_bf16 a[192:195], a[208:211], v[176:179], a[192:195]\n\t"
    "v_exp_f32 v204, v204\n\t"
    "v_mfma_f32_16x16x32_bf16 v[16:19], a[144:147], v[160:163], v[16:19]\n\t"
    "v_exp_f32 v205, v205\n\t"
    "v_mfma_f32_16x16x32_bf16 v[16:19], a[176:179], v[176:179], v[16:19]\n\t"
    "v_cvt_pk_bf16_f32 v142, v204, v205\n\t"
    "v_mfma_f32_16x16x32_bf16 v[48:51], a[144:147], v[164:167], v[48:51]\n\t"
    "ds_read_b64_tr_b16 a[160:161], v236 offset:8192\n\t"
    "ds_read_b64_tr_b16 a[162:163], v236 offset:12288\n\t"
    "v_mfma_f32_16x16x32_bf16 v[48:51], a[176:179], v[180:183], v[48:51]\n\t"
    "v_exp_f32 v206, v206\n\t"
    "v_mfma_f32_16x16x32_bf16 v[80:83], a[144:147], v[168:171], v[80:83]\n\t"
    "v_exp_f32 v207, v207\n\t"
    "v_mfma_f32_16x16x32_bf16 v[80:83], a[176:179], v[184:187], v[80:83]\n\t"
    "v_cvt_pk_bf16_f32 v143, v206, v207\n\t"
    "v_mfma_f32_16x16x32_bf16 v[112:115], a[144:147], v[172:175], v[112:115]\n\t"
    "buffer_load_dwordx4 v224, s[28:31], s62 offen nt lds\n\t"
    "v_mfma_f32_16x16x32_bf16 v[112:115], a[176:179], v[188:191], v[112:115]\n\t"
    "ds_read_b128 a[64:67], v232 offset:16384\n\t"
    "ds_read_b128 a[68:71], v233 offset:16384\n\t"
    "v_mfma_f32_16x16x32_bf16 v[200:203], a[96:99], a[16:19], v[212:215]\n\t"
    "v_exp_f32 v192, v192\n\t"
    "v_mfma_f32_16x16x32_bf16 v[200:203], a[100:103], a[20:23], v[200:203]\n\t"
    "v_exp_f32 v193, v193\n\t"
    "v_mfma_f32_16x16x32_bf16 v[200:203], a[104:107], a[24:27], v[200:203]\n\t"
    "v_cvt_pk_bf16_f32 v144, v192, v193\n\t"
    "v_mfma_f32_16x16x32_bf16 v[200:203], a[108:111], a[28:31], v[200:203]\n\t"
    "ds_read_b64_tr_b16 a[132:133], v237 offset:0\n\t"
    "ds_read_b64_tr_b16 a[134:135], v237 offset:4096\n\t"
    "v_mfma_f32_16x16x32_bf16 v[204:207], a[112:115], a[16:19], v[212:215]\n\t"
    "v_exp_f32 v194, v194\n\t"
    "v_mfma_f32_16x16x32_bf16 v[204:207], a[116:119], a[20:23], v[204:207]\n\t"
    "v_exp_f32 v195, v195\n\t"
    "v_mfma_f32_16x16x32_bf16 v[204:207], a[120:123], a[24:27], v[204:207]\n\t"
    "v_cvt_pk_bf16_f32 v145, v194, v195\n\t"
    "v_mfma_f32_16x16x32_bf16 v[204:207], a[124:127], a[28:31], v[204:207]\n\t"
    "buffer_load_dwordx4 v225, s[28:31], s62 offen offset:1024 nt lds\n\t"
    "v_mfma_f32_16x16x32_bf16 a[196:199], a[208:211], v[180:183], a[196:199]\n\t"
    "v_exp_f32 v196, v196\n\t"
    "v_mfma_f32_16x16x32_bf16 v[20:23], a[148:151], v[160:163], v[20:23]\n\t"
    "v_exp_f32 v197, v197\n\t"
    "v_mfma_f32_16x16x32_bf16 v[20:23], a[180:183], v[176:179], v[20:23]\n\t"
    "v_cvt_pk_bf16_f32 v146, v196, v197\n\t"
    "v_mfma_f32_16x16x32_bf16 v[52:55], a[148:151], v[164:167], v[52:55]\n\t"
    "ds_read_b64_tr_b16 a[164:165], v237 offset:8192\n\t"
    "ds_read_b64_tr_b16 a[166:167], v237 offset:12288\n\t"
    "v_mfma_f32_16x16x32_bf16 v[52:55], a[180:183], v[180:183], v[52:55]\n\t"
    "v_exp_f32 v198, v198\n\t"
    "v_mfma_f32_16x16x32_bf16 v[84:87], a[148:151], v[168:171], v[84:87]\n\t"
    "v_exp_f32 v199, v199\n\t"
    "v_mfma_f32_16x16x32_bf16 v[84:87], a[180:183], v[184:187], v[84:87]\n\t"
    "v_cvt_pk_bf16_f32 v147, v198, v199\n\t"
    "v_mfma_f32_16x16x32_bf16 v[116:119], a[148:151], v[172:175], v[116:119]\n\t"
    "s_cbranch_scc1 .Lpad1_%=\n\t"
    ".Lpadb1_%=:\n\t"
    "s_max_u32 s97, s97, 134\n\t"
    "v_mfma_f32_16x16x32_bf16 v[116:119], a[180:183], v[188:191], v[116:119]\n\t"
    "ds_read_b128 a[72:75], v234 offset:16384\n\t"
    "ds_read_b128 a[76:79], v235 offset:16384\n\t"
    "v_mfma_f32_16x16x32_bf16 v[192:195], a[96:99], a[32:35], v[216:219]\n\t"
    "v_exp_f32 v200, v200\n\t"
    "v_mfma_f32_16x16x32_bf16 v[192:195], a[100:103], a[36:39], v[192:195]\n\t"
    "v_exp_f32 v201, v201\n\t"
    "v_mfma_f32_16x16x32_bf16 v[192:195], a[104:107], a[40:43], v[192:195]\n\t"
    "v_cvt_pk_bf16_f32 v148, v200, v201\n\t"
    "v_mfma_f32_16x16x32_bf16 v[192:195], a[108:111], a[44:47], v[192:195]\n\t"
    "ds_read_b64_tr_b16 a[136:137], v238 offset:0\n\t"
    "ds_read_b64_tr_b16 a[138:139], v238 offset:4096\n\t"
    "v_mfma_f32_16x16x32_bf16 v[196:199], a[112:115], a[32:35], v[216:219]\n\t"
    "v_exp_f32 v202, v202\n\t"
    "v_mfma_f32_16x16x32_bf16 v[196:199], a[116:119], a[36:39], v[196:199]\n\t"
    "v_exp_f32 v203, v203\n\t"
    "v_mfma_f32_16x16x32_bf16 v[196:199], a[120:123], a[40:43], v[196:199]\n\t"
    "v_cvt_pk_bf16_f32 v149, v202, v203\n\t"
    "v_mfma_f32_16x16x32_bf16 v[196:199], a[124:127], a[44:47], v[196:199]\n\t"
    "buffer_load_dwordx4 v226, s[28:31], s62 offen offset:2048 nt lds\n\t"
    "v_mfma_f32_16x16x32_bf16 a[200:203], a[208:211], v[184:187], a[200:203]\n\t"
    "v_exp_f32 v204, v204\n\t"
    "v_mfma_f32_16x16x32_bf16 v[24:27], a[152:155], v[160:163], v[24:27]\n\t"
    "v_exp_f32 v205, v205\n\t"
    "v_mfma_f32_16x16x32_bf16 v[24:27], a[184:187], v[176:179], v[24:27]\n\t"
    "v_cvt_pk_bf16_f32 v150, v204, v205\n\t"
    "v_mfma_f32_16x16x32_bf16 v[56:59], a[152:155], v[164:167], v[56:59]\n\t"
    "ds_read_b64_tr_b16 a[168:169], v238 offset:8192\n\t"
    "ds_read_b64_tr_b16 a[170:171], v238 offset:12288\n\t"
    "v_mfma_f32_16x16x32_bf16 v[56:59], a[184:187], v[180:183], v[56:59]\n\t"
    "v_exp_f32 v206, v206\n\t"
    "v_mfma_f32_16x16x32_bf16 v[88:91], a[152:155], v[168:171], v[88:91]\n\t"
    "v_exp_f32 v207, v207\n\t"
    "v_mfma_f32_16x16x32_bf16 v[88:91], a[184:187], v[184:187], v[88:91]\n\t"
    "v_cvt_pk_bf16_f32 v151, v206, v207\n\t"
    "v_mfma_f32_16x16x32_bf16 v[120:123], a[152:155], v[172:175], v[120:123]\n\t"
    "buffer_load_dwordx4 v227, s[28:31], s62 offen offset:3072 nt lds\n\t"
    "v_mfma_f32_16x16x32_bf16 v[120:123], a[184:187], v[188:191], v[120:123]\n\t"
    "ds_read_b128 a[80:83], v232 offset:20480\n\t"
    "ds_read_b128 a[84:87], v233 offset:20480\n\t"
    "v_mfma_f32_16x16x32_bf16 v[200:203], a[96:99], a[48:51], v[220:223]\n\t"
    "v_exp_f32 v192, v192\n\t"
    "v_mfma_f32_16x16x32_bf16 v[200:203], a[100:103], a[52:55], v[200:203]\n\t"
    "v_exp_f32 v193, v193\n\t"
    "v_mfma_f32_16x16x32_bf16 v[200:203], a[104:107], a[56:59], v[200:203]\n\t"
    "v_cvt_pk_bf16_f32 v152, v192, v193\n\t"
    "v_mfma_f32_16x16x32_bf16 v[200:203], a[108:111], a[60:63], v[200:203]\n\t"
    "ds_read_b64_tr_b16 a[140:141], v239 offset:0\n\t"
    "ds_read_b64_tr_b16 a[142:143], v239 offset:4096\n\t"
    "v_mfma_f32_16x16x32_bf16 v[204:207], a[112:115], a[48:51], v[220:223]\n\t"
    "v_exp_f32 v194, v194\n\t"
    "v_mfma_f32_16x16x32_bf16 v[204:207], a[116:119], a[52:55], v[204:207]\n\t"
    "v_exp_f32 v195, v195\n\t"
    "v_mfma_f32_16x16x32_bf16 v[204:207], a[120:123], a[56:59], v[204:207]\n\t"
    "v_cvt_pk_bf16_f32 v153, v194, v195\n\t"
    "v_mfma_f32_16x16x32_bf16 v[204:207], a[124:127], a[60:63], v[204:207]\n\t"
    "ds_read_b64_tr_b16 a[172:173], v239 offset:8192\n\t"
    "ds_read_b64_tr_b16 a[174:175], v239 offset:12288\n\t"
    "v_mfma_f32_16x16x32_bf16 a[204:207], a[208:211], v[188:191], a[204:207]\n\t"
    "v_exp_f32 v196, v196\n\t"
    "v_mfma_f32_16x16x32_bf16 v[28:31], a[156:159], v[160:163], v[28:31]\n\t"
    "v_exp_f32 v197, v197\n\t"
    "v_mfma_f32_16x16x32_bf16 v[28:31], a[188:191], v[176:179], v[28:31]\n\t"
    "v_cvt_pk_bf16_f32 v154, v196, v197\n\t"
    "v_mfma_f32_16x16x32_bf16 v[60:63], a[156:159], v[164:167], v[60:63]\n\t"
    "ds_read_b128 a[88:91], v234 offset:20480\n\t"
    "ds_read_b128 a[92:95], v235 offset:20480\n\t"
    "v_mfma_f32_16x16x32_bf16 v[60:63], a[188:191], v[180:183], v[60:63]\n\t"
    "v_exp_f32 v198, v198\n\t"
    "v_mfma_f32_16x16x32_bf16 v[92:95], a[156:159], v[168:171], v[92:95]\n\t"
    "v_exp_f32 v199, v199\n\t"
    "v_mfma_f32_16x16x32_bf16 v[92:95], a[188:191], v[184:187], v[92:95]\n\t"
    "v_cvt_pk_bf16_f32 v155, v198, v199\n\t"
    "v_mfma_f32_16x16x32_bf16 v[124:127], a[156:159], v[172:175], v[124:127]\n\t"
    "s_add_u32 s62, s62, 0x4000\n\t"
    "s_sub_u32 s97, s97, 2\n\t"
    "v_mfma_f32_16x16x32_bf16 v[124:127], a[188:191], v[188:191], v[124:127]\n\t"
    "s_waitcnt lgkmcnt(0)\n\t"
    "v_mfma_f32_16x16x32_bf16 v[192:195], a[64:67], a[0:3], v[208:211]\n\t"
    "v_exp_f32 v200, v200\n\t"
    "v_mfma_f32_16x16x32_bf16 v[192:195], a[68:71], a[4:7], v[192:195]\n\t"
    "v_exp_f32 v201, v201\n\t"
    "v_mfma_f32_16x16x32_bf16 v[192:195], a[72:75], a[8:11], v[192:195]\n\t"
    "v_cvt_pk_bf16_f32 v156, v200, v201\n\t"
    "v_mfma_f32_16x16x32_bf16 v[192:195], a[76:79], a[12:15], v[192:195]\n\t"
    "ds_read_b64_tr_b16 a[144:145], v240 offset:0\n\t"
    "ds_read_b64_tr_b16 a[146:147], v240 offset:4096\n\t"
    "v_mfma_f32_16x16x32_bf16 v[196:199], a[80:83], a[0:3], v[208:211]\n\t"
    "v_exp_f32 v202, v202\n\t"
    "v_mfma_f32_16x16x32_bf16 v[196:199], a[84:87], a[4:7], v[196:199]\n\t"
    "v_exp_f32 v203, v203\n\t"
    "v_mfma_f32_16x16x32_bf16 v[196:199], a[88:91], a[8:11], v[196:199]\n\t"
    "v_cvt_pk_bf16_f32 v157, v202, v203\n\t"
    "v_mfma_f32_16x16x32_bf16 v[196:199], a[92:95], a[12:15], v[196:199]\n\t"
    "s_add_u32 m0, s73, 0x18000\n\t"
    "v_mfma_f32_16x16x32_bf16 a[192:195], a[208:211], v[128:131], a[192:195]\n\t"
    "v_exp_f32 v204, v204\n\t"
    "v_mfma_f32_16x16x32_bf16 v[0:3], a[128:131], v[128:131], v[0:3]\n\t"
    "v_exp_f32 v205, v205\n\t"
    "v_mfma_f32_16x16x32_bf16 v[0:3], a[160:163], v[144:147], v[0:3]\n\t"
    "v_cvt_pk_bf16_f32 v158, v204, v205\n\t"
    "v_mfma_f32_16x16x32_bf16 v[32:35], a[128:131], v[132:135], v[32:35]\n\t"
    "ds_read_b64_tr_b16 a[176:177], v240 offset:8192\n\t"
    "ds_read_b64_tr_b16 a[178:179], v240 offset:12288\n\t"
    "v_mfma_f32_16x16x32_bf16 v[32:35], a[160:163], v[148:151], v[32:35]\n\t"
    "v_exp_f32 v206, v206\n\t"
    "v_mfma_f32_16x16x32_bf16 v[64:67], a[128:131], v[136:139], v[64:67]\n\t"
    "v_exp_f32 v207, v207\n\t"
    "v_mfma_f32_16x16x32_bf16 v[64:67], a[160:163], v[152:155], v[64:67]\n\t"
    "v_cvt_pk_bf16_f32 v159, v206, v207\n\t"
    "v_mfma_f32_16x16x32_bf16 v[96:99], a[128:131], v[140:143], v[96:99]\n\t"
    "buffer_load_dwordx4 v228, s[32:35], s60 offen nt lds\n\t"
    "v_mfma_f32_16x16x32_bf16 v[96:99], a[160:163], v[156:159], v[96:99]\n\t"
    "ds_read_b128 a[96:99], v232 offset:24576\n\t"
    "ds_read_b128 a[100:103], v233 offset:24576\n\t"
    "v_mfma_f32_16x16x32_bf16 v[200:203], a[64:67], a[16:19], v[212:215]\n\t"
    "v_exp_f32 v192, v192\n\t"
    "v_mfma_f32_16x16x32_bf16 v[200:203], a[68:71], a[20:23], v[200:203]\n\t"
    "v_exp_f32 v193, v193\n\t"
    "v_mfma_f32_16x16x32_bf16 v[200:203], a[72:75], a[24:27], v[200:203]\n\t"
    "v_cvt_pk_bf16_f32 v160, v192, v193\n\t"
    "v_mfma_f32_16x16x32_bf16 v[200:203], a[76:79], a[28:31], v[200:203]\n\t"
    "ds_read_b64_tr_b16 a[148:149], v241 offset:0\n\t"
    "ds_read_b64_tr_b16 a[150:151], v241 offset:4096\n\t"
    "v_mfma_f32_16x16x32_bf16 v[204:207], a[80:83], a[16:19], v[212:215]\n\t"
    "v_exp_f32 v194, v194\n\t"
    "v_mfma_f32_16x16x32_bf16 v[204:207], a[84:87], a[20:23], v[204:207]\n\t"
    "v_exp_f32 v195, v195\n\t"
    "v_mfma_f32_16x16x32_bf16 v[204:207], a[88:91], a[24:27], v[204:207]\n\t"
    "v_cvt_pk_bf16_f32 v161, v194, v195\n\t"
    "v_mfma_f32_16x16x32_bf16 v[204:207], a[92:95], a[28:31], v[204:207]\n\t"
    "buffer_load_dwordx4 v229, s[32:35], s60 offen offset:1024 nt lds\n\t"
    "v_mfma_f32_16x16x32_bf16 a[196:199], a[208:211], v[132:135], a[196:199]\n\t"
    "v_exp_f32 v196, v196\n\t"
    "v_mfma_f32_16x16x32_bf16 v[4:7], a[132:135], v[128:131], v[4:7]\n\t"
    "v_exp_f32 v197, v197\n\t"
    "v_mfma_f32_16x16x32_bf16 v[4:7], a[164:167], v[144:147], v[4:7]\n\t"
    "v_cvt_pk_bf16_f32 v162, v196, v197\n\t"
    "v_mfma_f32_16x16x32_bf16 v[36:39], a[132:135], v[132:135], v[36:39]\n\t"
    "ds_read_b64_tr_b16 a[180:181], v241 offset:8192\n\t"
    "ds_read_b64_tr_b16 a[182:183], v241 offset:12288\n\t"
    "v_mfma_f32_16x16x32_bf16 v[36:39], a[164:167], v[148:151], v[36:39]\n\t"
    "v_exp_f32 v198, v198\n\t"
    "v_mfma_f32_16x16x32_bf16 v[68:71], a[132:135], v[136:139], v[68:71]\n\t"
    "v_exp_f32 v199, v199\n\t"
    "v_mfma_f32_16x16x32_bf16 v[68:71], a[164:167], v[152:155], v[68:71]\n\t"
    "v_cvt_pk_bf16_f32 v163, v198, v199\n\t"
    "v_mfma_f32_16x16x32_bf16 v[100:103], a[132:135], v[140:143], v[100:103]\n\t"
    "s_memrealtime s[98:99]\n\t"
    "s_add_u32 s96, s96, s97\n\t"
    "v_mfma_f32_16x16x32_bf16 v[100:103], a[164:167], v[156:159], v[100:103]\n\t"
    "ds_read_b128 a[104:107], v234 offset:24576\n\t"
    "ds_read_b128 a[108:111], v235 offset:24576\n\t"
    "v_mfma_f32_16x16x32_bf16 v[192:195], a[64:67], a[32:35], v[216:219]\n\t"
    "v_exp_f32 v200, v200\n\t"
    "v_mfma_f32_16x16x32_bf16 v[192:195], a[68:71], a[36:39], v[192:195]\n\t"
    "v_exp_f32 v201, v201\n\t"
    "v_mfma_f32_16x16x32_bf16 v[192:195], a[72:75], a[40:43], v[192:195]\n\t"
    "v_cvt_pk_bf16_f32 v164, v200, v201\n\t"
    "v_mfma_f32_16x16x32_bf16 v[192:195], a[76:79], a[44:47], v[192:195]\n\t"
    "ds_read_b64_tr_b16 a[152:153], v242 offset:0\n\t"
    "ds_read_b64_tr_b16 a[154:155], v242 offset:4096\n\t"
    "v_mfma_f32_16x16x32_bf16 v[196:199], a[80:83], a[32:35], v[216:219]\n\t"
    "v_exp_f32 v202, v202\n\t"
    "v_mfma_f32_16x16x32_bf16 v[196:199], a[84:87], a[36:39], v[196:199]\n\t"
    "v_exp_f32 v203, v203\n\t"
    "v_mfma_f32_16x16x32_bf16 v[196:199], a[88:91], a[40:43], v[196:199]\n\t"
    "v_cvt_pk_bf16_f32 v165, v202, v203\n\t"
    "v_mfma_f32_16x16x32_bf16 v[196:199], a[92:95], a[44:47], v[196:199]\n\t"
    "buffer_load_dwordx4 v228, s[32:35], s60 offen offset:2048 nt lds\n\t"
    "v_mfma_f32_16x16x32_bf16 a[200:203], a[208:211], v[136:139], a[200:203]\n\t"
    "v_exp_f32 v204, v204\n\t"
    "v_mfma_f32_16x16x32_bf16 v[8:11], a[136:139], v[128:131], v[8:11]\n\t"
    "v_exp_f32 v205, v205\n\t"
    "v_mfma_f32_16x16x32_bf16 v[8:11], a[168:171], v[144:147], v[8:11]\n\t"
    "v_cvt_pk_bf16_f32 v166, v204, v205\n\t"
    "v_mfma_f32_16x16x32_bf16 v[40:43], a[136:139], v[132:135], v[40:43]\n\t"
    "ds_read_b64_tr_b16 a[184:185], v242 offset:8192\n\t"
    "ds_read_b64_tr_b16 a[186:187], v242 offset:12288\n\t"
    "v_mfma_f32_16x16x32_bf16 v[40:43], a[168:171], v[148:151], v[40:43]\n\t"
    "v_exp_f32 v206, v206\n\t"
    "v_mfma_f32_16x16x32_bf16 v[72:75], a[136:139], v[136:139], v[72:75]\n\t"
    "v_exp_f32 v207, v207\n\t"
    "v_mfma_f32_16x16x32_bf16 v[72:75], a[168:171], v[152:155], v[72:75]\n\t"
    "v_cvt_pk_bf16_f32 v167, v206, v207\n\t"
    "v_mfma_f32_16x16x32_bf16 v[104:107], a[136:139], v[140:143], v[104:107]\n\t"
    "buffer_load_dwordx4 v229, s[32:35], s60 offen offset:3072 nt lds\n\t"
    "v_mfma_f32_16x16x32_bf16 v[104:107], a[168:171], v[156:159], v[104:107]\n\t"
    "ds_read_b128 a[112:115], v232 offset:28672\n\t"
    "ds_read_b128 a[116:119], v233 offset:28672\n\t"
    "v_mfma_f32_16x16x32_bf16 v[200:203], a[64:67], a[48:51], v[220:223]\n\t"
    "v_exp_f32 v192, v192\n\t"
    "v_mfma_f32_16x16x32_bf16 v[200:203], a[68:71], a[52:55], v[200:203]\n\t"
    "v_exp_f32 v193, v193\n\t"
    "v_mfma_f32_16x16x32_bf16 v[200:203], a[72:75], a[56:59], v[200:203]\n\t"
    "v_cvt_pk_bf16_f32 v168, v192, v193\n\t"
    "v_mfma_f32_16x16x32_bf16 v[200:203], a[76:79], a[60:63], v[200:203]\n\t"
    "ds_read_b64_tr_b16 a[156:157], v243 offset:0\n\t"
    "ds_read_b64_tr_b16 a[158:159], v243 offset:4096\n\t"
    "v_mfma_f32_16x16x32_bf16 v[204:207], a[80:83], a[48:51], v[220:223]\n\t"
    "v_exp_f32 v194, v194\n\t"
    "v_mfma_f32_16x16x32_bf16 v[204:207], a[84:87], a[52:55], v[204:207]\n\t"
    "v_exp_f32 v195, v195\n\t"
    "v_mfma_f32_16x16x32_bf16 v[204:207], a[88:91], a[56:59], v[204:207]\n\t"
    "v_cvt_pk_bf16_f32 v169, v194, v195\n\t"
    "v_mfma_f32_16x16x32_bf16 v[204:207], a[92:95], a[60:63], v[204:207]\n\t"
    "ds_read_b64_tr_b16 a[188:189], v243 offset:8192\n\t"
    "ds_read_b64_tr_b16 a[190:191], v243 offset:12288\n\t"
    "v_mfma_f32_16x16x32_bf16 a[204:207], a[208:211], v[140:143], a[204:207]\n\t"
    "v_exp_f32 v196, v196\n\t"
    "v_mfma_f32_16x16x32_bf16 v[12:15], a[140:143], v[128:131], v[12:15]\n\t"
    "v_exp_f32 v197, v197\n\t"
    "v_mfma_f32_16x16x32_bf16 v[12:15], a[172:175], v[144:147], v[12:15]\n\t"
    "v_cvt_pk_bf16_f32 v170, v196, v197\n\t"
    "v_mfma_f32_16x16x32_bf16 v[44:47], a[140:143], v[132:135], v[44:47]\n\t"
    "ds_read_b128 a[120:123], v234 offset:28672\n\t"
    "ds_read_b128 a[124:127], v235 offset:28672\n\t"
    "v_mfma_f32_16x16x32_bf16 v[44:47], a[172:175], v[148:151], v[44:47]\n\t"
    "v_exp_f32 v198, v198\n\t"
    "v_mfma_f32_16x16x32_bf16 v[76:79], a[140:143], v[136:139], v[76:79]\n\t"
    "v_exp_f32 v199, v199\n\t"
    "v_mfma_f32_16x16x32_bf16 v[76:79], a[172:175], v[152:155], v[76:79]\n\t"
    "v_cvt_pk_bf16_f32 v171, v198, v199\n\t"
    "v_mfma_f32_16x16x32_bf16 v[108:111], a[140:143], v[140:143], v[108:111]\n\t"
    "s_add_u32 s60, s60, 0x4000\n\t"
    "v_mfma_f32_16x16x32_bf16 v[108:111], a[172:175], v[156:159], v[108:111]\n\t"
    "s_waitcnt vmcnt(8) lgkmcnt(0)\n\t"
    "s_barrier\n\t"
    "v_mfma_f32_16x16x32_bf16 v[192:195], a[96:99], a[0:3], v[208:211]\n\t"
    "v_exp_f32 v200, v200\n\t"
    "v_mfma_f32_16x16x32_bf16 v[192:195], a[100:103], a[4:7], v[192:195]\n\t"
    "v_exp_f32 v201, v201\n\t"
    "v_mfma_f32_16x16x32_bf16 v[192:195], a[104:107], a[8:11], v[192:195]\n\t"
    "v_cvt_pk_bf16_f32 v172, v200, v201\n\t"
    "v_mfma_f32_16x16x32_bf16 v[192:195], a[108:111], a[12:15], v[192:195]\n\t"
    "ds_read_b64_tr_b16 a[128:129], v236 offset:16384\n\t"
    "ds_read_b64_tr_b16 a[130:131], v236 offset:20480\n\t"
    "v_mfma_f32_16x16x32_bf16 v[196:199], a[112:115], a[0:3], v[208:211]\n\t"
    "v_exp_f32 v202, v202\n\t"
    "v_mfma_f32_16x16x32_bf16 v[196:199], a[116:119], a[4:7], v[196:199]\n\t"
    "v_exp_f32 v203, v203\n\t"
    "v_mfma_f32_16x16x32_bf16 v[196:199], a[120:123], a[8:11], v[196:199]\n\t"
    "v_cvt_pk_bf16_f32 v173, v202, v203\n\t"
    "v_mfma_f32_16x16x32_bf16 v[196:199], a[124:127], a[12:15], v[196:199]\n\t"
    "s_add_u32 m0, s73, 0x0\n\t"
    "s_cmp_lt_u32 s98, s96\n\t"
    "v_mfma_f32_16x16x32_bf16 a[192:195], a[208:211], v[144:147], a[192:195]\n\t"
    "v_exp_f32 v204, v204\n\t"
    "v_mfma_f32_16x16x32_bf16 v[16:19], a[144:147], v[128:131], v[16:19]\n\t"
    "v_exp_f32 v205, v205\n\t"
    "v_mfma_f32_16x16x32_bf16 v[16:19], a[176:179], v[144:147], v[16:19]\n\t"
    "v_cvt_pk_bf16_f32 v174, v204, v205\n\t"
    "v_mfma_f32_16x16x32_bf16 v[48:51], a[144:147], v[132:135], v[48:51]\n\t"
    "ds_read_b64_tr_b16 a[160:161], v236 offset:24576\n\t"
    "ds_read_b64_tr_b16 a[162:163], v236 offset:28672\n\t"
    "v_mfma_f32_16x16x32_bf16 v[48:51], a[176:179], v[148:151], v[48:51]\n\t"
    "v_exp_f32 v206, v206\n\t"
    "v_mfma_f32_16x16x32_bf16 v[80:83], a[144:147], v[136:139], v[80:83]\n\t"
    "v_exp_f32 v207, v207\n\t"
    "v_mfma_f32_16x16x32_bf16 v[80:83], a[176:179], v[152:155], v[80:83]\n\t"
    "v_cvt_pk_bf16_f32 v175, v206, v207\n\t"
    "v_mfma_f32_16x16x32_bf16 v[112:115], a[144:147], v[140:143], v[112:115]\n\t"
    "buffer_load_dwordx4 v224, s[28:31], s62 offen nt lds\n\t"
    "v_mfma_f32_16x16x32_bf16 v[112:115], a[176:179], v[156:159], v[112:115]\n\t"
    "ds_read_b128 a[64:67], v232 offset:32768\n\t"
    "ds_read_b128 a[68:71], v233 offset:32768\n\t"
    "v_mfma_f32_16x16x32_bf16 v[200:203], a[96:99], a[16:19], v[212:215]\n\t"
    "v_exp_f32 v192, v192\n\t"
    "v_mfma_f32_16x16x32_bf16 v[200:203], a[100:103], a[20:23], v[200:203]\n\t"
    "v_exp_f32 v193, v193\n\t"
    "v_mfma_f32_16x16x32_bf16 v[200:203], a[104:107], a[24:27], v[200:203]\n\t"
    "v_cvt_pk_bf16_f32 v176, v192, v193\n\t"
    "v_mfma_f32_16x16x32_bf16 v[200:203], a[108:111], a[28:31], v[200:203]\n\t"
    "ds_read_b64_tr_b16 a[132:133], v237 offset:16384\n\t"
    "ds_read_b64_tr_b16 a[134:135], v237 offset:20480\n\t"
    "v_mfma_f32_16x16x32_bf16 v[204:207], a[112:115], a[16:19], v[212:215]\n\t"
    "v_exp_f32 v194, v194\n\t"
    "v_mfma_f32_16x16x32_bf16 v[204:207], a[116:119], a[20:23], v[204:207]\n\t"
    "v_exp_f32 v195, v195\n\t"
    "v_mfma_f32_16x16x32_bf16 v[204:207], a[120:123], a[24:27], v[204:207]\n\t"
    "v_cvt_pk_bf16_f32 v177, v194, v195\n\t"
    "v_mfma_f32_16x16x32_bf16 v[204:207], a[124:127], a[28:31], v[204:207]\n\t"
    "buffer_load_dwordx4 v225, s[28:31], s62 offen offset:1024 nt lds\n\t"
    "v_mfma_f32_16x16x32_bf16 a[196:199], a[208:211], v[148:151], a[196:199]\n\t"
    "v_exp_f32 v196, v196\n\t"
    "v_mfma_f32_16x16x32_bf16 v[20:23], a[148:151], v[128:131], v[20:23]\n\t"
    "v_exp_f32 v197, v197\n\t"
    "v_mfma_f32_16x16x32_bf16 v[20:23], a[180:183], v[144:147], v[20:23]\n\t"
    "v_cvt_pk_bf16_f32 v178, v196, v197\n\t"
    "v_mfma_f32_16x16x32_bf16 v[52:55], a[148:151], v[132:135], v[52:55]\n\t"
    "ds_read_b64_tr_b16 a[164:165], v237 offset:24576\n\t"
    "ds_read_b64_tr_b16 a[166:167], v237 offset:28672\n\t"
    "v_mfma_f32_16x16x32_bf16 v[52:55], a[180:183], v[148:151], v[52:55]\n\t"
    "v_exp_f32 v198, v198\n\t"
    "v_mfma_f32_16x16x32_bf16 v[84:87], a[148:151], v[136:139], v[84:87]\n\t"
    "v_exp_f32 v199, v199\n\t"
    "v_mfma_f32_16x16x32_bf16 v[84:87], a[180:183], v[152:155], v[84:87]\n\t"
    "v_cvt_pk_bf16_f32 v179, v198, v199\n\t"
    "v_mfma_f32_16x16x32_bf16 v[116:119], a[148:151], v[140:143], v[116:119]\n\t"
    "s_cbranch_scc1 .Lpad3_%=\n\t"
    ".Lpadb3_%=:\n\t"
    "s_max_u32 s97, s97, 134\n\t"
    "v_mfma_f32_16x16x32_bf16 v[116:119], a[180:183], v[156:159], v[116:119]\n\t"
    "ds_read_b128 a[72:75], v234 offset:32768\n\t"
    "ds_read_b128 a[76:79], v235 offset:32768\n\t"
    "v_mfma_f32_16x16x32_bf16 v[192:195], a[96:99], a[32:35], v[216:219]\n\t"
    "v_exp_f32 v200, v200\n\t"
    "v_mfma_f32_16x16x32_bf16 v[192:195], a[100:103], a[36:39], v[192:195]\n\t"
    "v_exp_f32 v201, v201\n\t"
    "v_mfma_f32_16x16x32_bf16 v[192:195], a[104:107], a[40:43], v[192:195]\n\t"
    "v_cvt_pk_bf16_f32 v180, v200, v201\n\t"
    "v_mfma_f32_16x16x32_bf16 v[192:195], a[108:111], a[44:47], v[192:195]\n\t"
    "ds_read_b64_tr_b16 a[136:137], v238 offset:16384\n\t"
    "ds_read_b64_tr_b16 a[138:139], v238 offset:20480\n\t"
    "v_mfma_f32_16x16x32_bf16 v[196:199], a[112:115], a[32:35], v[216:219]\n\t"
    "v_exp_f32 v202, v202\n\t"
    "v_mfma_f32_16x16x32_bf16 v[196:199], a[116:119], a[36:39], v[196:199]\n\t"
    "v_exp_f32 v203, v203\n\t"
    "v_mfma_f32_16x16x32_bf16 v[196:199], a[120:123], a[40:43], v[196:199]\n\t"
    "v_cvt_pk_bf16_f32 v181, v202, v203\n\t"
    "v_mfma_f32_16x16x32_bf16 v[196:199], a[124:127], a[44:47], v[196:199]\n\t"
    "buffer_load_dwordx4 v226, s[28:31], s62 offen offset:2048 nt lds\n\t"
    "v_mfma_f32_16x16x32_bf16 a[200:203], a[208:211], v[152:155], a[200:203]\n\t"
    "v_exp_f32 v204, v204\n\t"
    "v_mfma_f32_16x16x32_bf16 v[24:27], a[152:155], v[128:131], v[24:27]\n\t"
    "v_exp_f32 v205, v205\n\t"
    "v_mfma_f32_16x16x32_bf16 v[24:27], a[184:187], v[144:147], v[24:27]\n\t"
    "v_cvt_pk_bf16_f32 v182, v204, v205\n\t"
    "v_mfma_f32_16x16x32_bf16 v[56:59], a[152:155], v[132:135], v[56:59]\n\t"
    "ds_read_b64_tr_b16 a[168:169], v238 offset:24576\n\t"
    "ds_read_b64_tr_b16 a[170:171], v238 offset:28672\n\t"
    "v_mfma_f32_16x16x32_bf16 v[56:59], a[184:187], v[148:151], v[56:59]\n\t"
    "v_exp_f32 v206, v206\n\t"
    "v_mfma_f32_16x16x32_bf16 v[88:91], a[152:155], v[136:139], v[88:91]\n\t"
    "v_exp_f32 v207, v207\n\t"
    "v_mfma_f32_16x16x32_bf16 v[88:91], a[184:187], v[152:155], v[88:91]\n\t"
    "v_cvt_pk_bf16_f32 v183, v206, v207\n\t"
    "v_mfma_f32_16x16x32_bf16 v[120:123], a[152:155], v[140:143], v[120:123]\n\t"
    "buffer_load_dwordx4 v227, s[28:31], s62 offen offset:3072 nt lds\n\t"
    "v_mfma_f32_16x16x32_bf16 v[120:123], a[184:187], v[156:159], v[120:123]\n\t"
    "ds_read_b128 a[80:83], v232 offset:36864\n\t"
    "ds_read_b128 a[84:87], v233 offset:36864\n\t"
    "v_mfma_f32_16x16x32_bf16 v[200:203], a[96:99], a[48:51], v[220:223]\n\t"
    "v_exp_f32 v192, v192\n\t"
    "v_mfma_f32_16x16x32_bf16 v[200:203], a[100:103], a[52:55], v[200:203]\n\t"
    "v_exp_f32 v193, v193\n\t"
    "v_mfma_f32_16x16x32_bf16 v[200:203], a[104:107], a[56:59], v[200:203]\n\t"
    "v_cvt_pk_bf16_f32 v184, v192, v193\n\t"
    "v_mfma_f32_16x16x32_bf16 v[200:203], a[108:111], a[60:63], v[200:203]\n\t"
    "ds_read_b64_tr_b16 a[140:141], v239 offset:16384\n\t"
    "ds_read_b64_tr_b16 a[142:143], v239 offset:20480\n\t"
    "v_mfma_f32_16x16x32_bf16 v[204:207], a[112:115], a[48:51], v[220:223]\n\t"
    "v_exp_f32 v194, v194\n\t"
    "v_mfma_f32_16x16x32_bf16 v[204:207], a[116:119], a[52:55], v[204:207]\n\t"
    "v_exp_f32 v195, v195\n\t"
    "v_mfma_f32_16x16x32_bf16 v[204:207], a[120:123], a[56:59], v[204:207]\n\t"
    "v_cvt_pk_bf16_f32 v185, v194, v195\n\t"
    "v_mfma_f32_16x16x32_bf16 v[204:207], a[124:127], a[60:63], v[204:207]\n\t"
    "ds_read_b64_tr_b16 a[172:173], v239 offset:24576\n\t"
    "ds_read_b64_tr_b16 a[174:175], v239 offset:28672\n\t"
    "v_mfma_f32_16x16x32_bf16 a[204:207], a[208:211], v[156:159], a[204:207]\n\t"
    "v_exp_f32 v196, v196\n\t"
    "v_mfma_f32_16x16x32_bf16 v[28:31], a[156:159], v[128:131], v[28:31]\n\t"
    "v_exp_f32 v197, v197\n\t"
    "v_mfma_f32_16x16x32_bf16 v[28:31], a[188:191], v[144:147], v[28:31]\n\t"
    "v_cvt_pk_bf16_f32 v186, v196, v197\n\t"
    "v_mfma_f32_16x16x32_bf16 v[60:63], a[156:159], v[132:135], v[60:63]\n\t"
    "ds_read_b128 a[88:91], v234 offset:36864\n\t"
    "ds_read_b128 a[92:95], v235 offset:36864\n\t"
    "v_mfma_f32_16x16x32_bf16 v[60:63], a[188:191], v[148:151], v[60:63]\n\t"
    "v_exp_f32 v198, v198\n\t"
    "v_mfma_f32_16x16x32_bf16 v[92:95], a[156:159], v[136:139], v[92:95]\n\t"
    "v_exp_f32 v199, v199\n\t"
    "v_mfma_f32_16x16x32_bf16 v[92:95], a[188:191], v[152:155], v[92:95]\n\t"
    "v_cvt_pk_bf16_f32 v187, v198, v199\n\t"
    "v_mfma_f32_16x16x32_bf16 v[124:127], a[156:159], v[140:143], v[124:127]\n\t"
    "s_add_u32 s62, s62, 0x4000\n\t"
    "s_sub_u32 s97, s97, 2\n\t"
    "v_mfma_f32_16x16x32_bf16 v[124:127], a[188:191], v[156:159], v[124:127]\n\t"
    "s_waitcnt lgkmcnt(0)\n\t"
    "v_mfma_f32_16x16x32_bf16 v[192:195], a[64:67], a[0:3], v[208:211]\n\t"
    "v_exp_f32 v200, v200\n\t"
    "v_mfma_f32_16x16x32_bf16 v[192:195], a[68:71], a[4:7], v[192:195]\n\t"
    "v_exp_f32 v201, v201\n\t"
    "v_mfma_f32_16x16x32_bf16 v[192:195], a[72:75], a[8:11], v[192:195]\n\t"
    "v_cvt_pk_bf16_f32 v188, v200, v201\n\t"
    "v_mfma_f32_16x16x32_bf16 v[192:195], a[76:79], a[12:15], v[192:195]\n\t"
    "ds_read_b64_tr_b16 a[144:145], v240 offset:16384\n\t"
    "ds_read_b64_tr_b16 a[146:147], v240 offset:20480\n\t"
    "v_mfma_f32_16x16x32_bf16 v[196:199], a[80:83], a[0:3], v[208:211]\n\t"
    "v_exp_f32 v202, v202\n\t"
    "v_mfma_f32_16x16x32_bf16 v[196:199], a[84:87], a[4:7], v[196:199]\n\t"
    "v_exp_f32 v203, v203\n\t"
    "v_mfma_f32_16x16x32_bf16 v[196:199], a[88:91], a[8:11], v[196:199]\n\t"
    "v_cvt_pk_bf16_f32 v189, v202, v203\n\t"
    "v_mfma_f32_16x16x32_bf16 v[196:199], a[92:95], a[12:15], v[196:199]\n\t"
    "s_add_u32 m0, s73, 0x1c000\n\t"
    "v_mfma_f32_16x16x32_bf16 a[192:195], a[208:211], v[160:163], a[192:195]\n\t"
    "v_exp_f32 v204, v204\n\t"
    "v_mfma_f32_16x16x32_bf16 v[0:3], a[128:131], v[160:163], v[0:3]\n\t"
    "v_exp_f32 v205, v205\n\t"
    "v_mfma_f32_16x16x32_bf16 v[0:3], a[160:163], v[176:179], v[0:3]\n\t"
    "v_cvt_pk_bf16_f32 v190, v204, v205\n\t"
    "v_mfma_f32_16x16x32_bf16 v[32:35], a[128:131], v[164:167], v[32:35]\n\t"
    "ds_read_b64_tr_b16 a[176:177], v240 offset:24576\n\t"
    "ds_read_b64_tr_b16 a[178:179], v240 offset:28672\n\t"
    "v_mfma_f32_16x16x32_bf16 v[32:35], a[160:163], v[180:183], v[32:35]\n\t"
    "v_exp_f32 v206, v206\n\t"
    "v_mfma_f32_16x16x32_bf16 v[64:67], a[128:131], v[168:171], v[64:67]\n\t"
    "v_exp_f32 v207, v207\n\t"
    "v_mfma_f32_16x16x32_bf16 v[64:67], a[160:163], v[184:187], v[64:67]\n\t"
    "v_cvt_pk_bf16_f32 v191, v206, v207\n\t"
    "v_mfma_f32_16x16x32_bf16 v[96:99], a[128:131], v[172:175], v[96:99]\n\t"
    "buffer_load_dwordx4 v228, s[32:35], s60 offen nt lds\n\t"
    "v_mfma_f32_16x16x32_bf16 v[96:99], a[160:163], v[188:191], v[96:99]\n\t"
    "ds_read_b128 a[96:99], v232 offset:40960\n\t"
    "ds_read_b128 a[100:103], v233 offset:40960\n\t"
    "v_mfma_f32_16x16x32_bf16 v[200:203], a[64:67], a[16:19], v[212:215]\n\t"
    "v_exp_f32 v192, v192\n\t"
    "v_mfma_f32_16x16x32_bf16 v[200:203], a[68:71], a[20:23], v[200:203]\n\t"
    "v_exp_f32 v193, v193\n\t"
    "v_mfma_f32_16x16x32_bf16 v[200:203], a[72:75], a[24:27], v[200:203]\n\t"
    "v_cvt_pk_bf16_f32 v128, v192, v193\n\t"
    "v_mfma_f32_16x16x32_bf16 v[200:203], a[76:79], a[28:31], v[200:203]\n\t"
    "ds_read_b64_tr_b16 a[148:149], v241 offset:16384\n\t"
    "ds_read_b64_tr_b16 a[150:151], v241 offset:20480\n\t"
    "v_mfma_f32_16x16x32_bf16 v[204:207], a[80:83], a[16:19], v[212:215]\n\t"
    "v_exp_f32 v194, v194\n\t"
    "v_mfma_f32_16x16x32_bf16 v[204:207], a[84:87], a[20:23], v[204:207]\n\t"
    "v_exp_f32 v195, v195\n\t"
    "v_mfma_f32_16x16x32_bf16 v[204:207], a[88:91], a[24:27], v[204:207]\n\t"
    "v_cvt_pk_bf16_f32 v129, v194, v195\n\t"
    "v_mfma_f32_16x16x32_bf16 v[204:207], a[92:95], a[28:31], v[204:207]\n\t"
    "buffer_load_dwordx4 v229, s[32:35], s60 offen offset:1024 nt lds\n\t"
    "v_mfma_f32_16x16x32_bf16 a[196:199], a[208:211], v[164:167], a[196:199]\n\t"
    "v_exp_f32 v196, v196\n\t"
    "v_mfma_f32_16x16x32_bf16 v[4:7], a[132:135], v[160:163], v[4:7]\n\t"
    "v_exp_f32 v197, v197\n\t"
    "v_mfma_f32_16x16x32_bf16 v[4:7], a[164:167], v[176:179], v[4:7]\n\t"
    "v_cvt_pk_bf16_f32 v130, v196, v197\n\t"
    "v_mfma_f32_16x16x32_bf16 v[36:39], a[132:135], v[164:167], v[36:39]\n\t"
    "ds_read_b64_tr_b16 a[180:181], v241 offset:24576\n\t"
    "ds_read_b64_tr_b16 a[182:183], v241 offset:28672\n\t"
    "v_mfma_f32_16x16x32_bf16 v[36:39], a[164:167], v[180:183], v[36:39]\n\t"
    "v_exp_f32 v198, v198\n\t"
    "v_mfma_f32_16x16x32_bf16 v[68:71], a[132:135], v[168:171], v[68:71]\n\t"
    "v_exp_f32 v199, v199\n\t"
    "v_mfma_f32_16x16x32_bf16 v[68:71], a[164:167], v[184:187], v[68:71]\n\t"
    "v_cvt_pk_bf16_f32 v131, v198, v199\n\t"
    "v_mfma_f32_16x16x32_bf16 v[100:103], a[132:135], v[172:175], v[100:103]\n\t"
    "s_memrealtime s[98:99]\n\t"
    "s_add_u32 s96, s96, s97\n\t"
    "v_mfma_f32_16x16x32_bf16 v[100:103], a[164:167], v[188:191], v[100:103]\n\t"
    "ds_read_b128 a[104:107], v234 offset:40960\n\t"
    "ds_read_b128 a[108:111], v235 offset:40960\n\t"
    "v_mfma_f32_16x16x32_bf16 v[192:195], a[64:67], a[32:35], v[216:219]\n\t"
    "v_exp_f32 v200, v200\n\t"
    "v_mfma_f32_16x16x32_bf16 v[192:195], a[68:71], a[36:39], v[192:195]\n\t"
    "v_exp_f32 v201, v201\n\t"
    "v_mfma_f32_16x16x32_bf16 v[192:195], a[72:75], a[40:43], v[192:195]\n\t"
    "v_cvt_pk_bf16_f32 v132, v200, v201\n\t"
    "v_mfma_f32_16x16x32_bf16 v[192:195], a[76:79], a[44:47], v[192:195]\n\t"
    "ds_read_b64_tr_b16 a[152:153], v242 offset:16384\n\t"
    "ds_read_b64_tr_b16 a[154:155], v242 offset:20480\n\t"
    "v_mfma_f32_16x16x32_bf16 v[196:199], a[80:83], a[32:35], v[216:219]\n\t"
    "v_exp_f32 v202, v202\n\t"
    "v_mfma_f32_16x16x32_bf16 v[196:199], a[84:87], a[36:39], v[196:199]\n\t"
    "v_exp_f32 v203, v203\n\t"
    "v_mfma_f32_16x16x32_bf16 v[196:199], a[88:91], a[40:43], v[196:199]\n\t"
    "v_cvt_pk_bf16_f32 v133, v202, v203\n\t"
    "v_mfma_f32_16x16x32_bf16 v[196:199], a[92:95], a[44:47], v[196:199]\n\t"
    "buffer_load_dwordx4 v228, s[32:35], s60 offen offset:2048 nt lds\n\t"
    "v_mfma_f32_16x16x32_bf16 a[200:203], a[208:211], v[168:171], a[200:203]\n\t"
    "v_exp_f32 v204, v204\n\t"
    "v_mfma_f32_16x16x32_bf16 v[8:11], a[136:139], v[160:163], v[8:11]\n\t"
    "v_exp_f32 v205, v205\n\t"
    "v_mfma_f32_16x16x32_bf16 v[8:11], a[168:171], v[176:179], v[8:11]\n\t"
    "v_cvt_pk_bf16_f32 v134, v204, v205\n\t"
    "v_mfma_f32_16x16x32_bf16 v[40:43], a[136:139], v[164:167], v[40:43]\n\t"
    "ds_read_b64_tr_b16 a[184:185], v242 offset:24576\n\t"
    "ds_read_b64_tr_b16 a[186:187], v242 offset:28672\n\t"
    "v_mfma_f32_16x16x32_bf16 v[40:43], a[168:171], v[180:183], v[40:43]\n\t"
    "v_exp_f32 v206, v206\n\t"
    "v_mfma_f32_16x16x32_bf16 v[72:75], a[136:139], v[168:171], v[72:75]\n\t"
    "v_exp_f32 v207, v207\n\t"
    "v_mfma_f32_16x16x32_bf16 v[72:75], a[168:171], v[184:187], v[72:75]\n\t"
    "v_cvt_pk_bf16_f32 v135, v206, v207\n\t"
    "v_mfma_f32_16x16x32_bf16 v[104:107], a[136:139], v[172:175], v[104:107]\n\t"
    "buffer_load_dwordx4 v229, s[32:35], s60 offen offset:3072 nt lds\n\t"
    "v_mfma_f32_16x16x32_bf16 v[104:107], a[168:171], v[188:191], v[104:107]\n\t"
    "ds_read_b128 a[112:115], v232 offset:45056\n\t"
    "ds_read_b128 a[116:119], v233 offset:45056\n\t"
    "v_mfma_f32_16x16x32_bf16 v[200:203], a[64:67], a[48:51], v[220:223]\n\t"
    "v_exp_f32 v192, v192\n\t"
    "v_mfma_f32_16x16x32_bf16 v[200:203], a[68:71], a[52:55], v[200:203]\n\t"
    "v_exp_f32 v193, v193\n\t"
    "v_mfma_f32_16x16x32_bf16 v[200:203], a[72:75], a[56:59], v[200:203]\n\t"
    "v_cvt_pk_bf16_f32 v136, v192, v193\n\t"
    "v_mfma_f32_16x16x32_bf16 v[200:203], a[76:79], a[60:63], v[200:203]\n\t"
    "ds_read_b64_tr_b16 a[156:157], v243 offset:16384\n\t"
    "ds_read_b64_tr_b16 a[158:159], v243 offset:20480\n\t"
    "v_mfma_f32_16x16x32_bf16 v[204:207], a[80:83], a[48:51], v[220:223]\n\t"
    "v_exp_f32 v194, v194\n\t"
    "v_mfma_f32_16x16x32_bf16 v[204:207], a[84:87], a[52:55], v[204:207]\n\t"
    "v_exp_f32 v195, v195\n\t"
    "v_mfma_f32_16x16x32_bf16 v[204:207], a[88:91], a[56:59], v[204:207]\n\t"
    "v_cvt_pk_bf16_f32 v137, v194, v195\n\t"
    "v_mfma_f32_16x16x32_bf16 v[204:207], a[92:95], a[60:63], v[204:207]\n\t"
    "ds_read_b64_tr_b16 a[188:189], v243 offset:24576\n\t"
    "ds_read_b64_tr_b16 a[190:191], v243 offset:28672\n\t"
    "v_mfma_f32_16x16x32_bf16 a[204:207], a[208:211], v[172:175], a[204:207]\n\t"
    "v_exp_f32 v196, v196\n\t"
    "v_mfma_f32_16x16x32_bf16 v[12:15], a[140:143], v[160:163], v[12:15]\n\t"
    "v_exp_f32 v197, v197\n\t"
    "v_mfma_f32_16x16x32_bf16 v[12:15], a[172:175], v[176:179], v[12:15]\n\t"
    "v_cvt_pk_bf16_f32 v138, v196, v197\n\t"
    "v_mfma_f32_16x16x32_bf16 v[44:47], a[140:143], v[164:167], v[44:47]\n\t"
    "ds_read_b128 a[120:123], v234 offset:45056\n\t"
    "ds_read_b128 a[124:127], v235 offset:45056\n\t"
    "v_mfma_f32_16x16x32_bf16 v[44:47], a[172:175], v[180:183], v[44:47]\n\t"
    "v_exp_f32 v198, v198\n\t"
    "v_mfma_f32_16x16x32_bf16 v[76:79], a[140:143], v[168:171], v[76:79]\n\t"
    "v_exp_f32 v199, v199\n\t"
    "v_mfma_f32_16x16x32_bf16 v[76:79], a[172:175], v[184:187], v[76:79]\n\t"
    "v_cvt_pk_bf16_f32 v139, v198, v199\n\t"
    "v_mfma_f32_16x16x32_bf16 v[108:111], a[140:143], v[172:175], v[108:111]\n\t"
    "s_add_u32 s60, s60, 0x4000\n\t"
    "v_mfma_f32_16x16x32_bf16 v[108:111], a[172:175], v[188:191], v[108:111]\n\t"
    "s_waitcnt vmcnt(8) lgkmcnt(0)\n\t"
    "s_barrier\n\t"
    "v_mfma_f32_16x16x32_bf16 v[192:195], a[96:99], a[0:3], v[208:211]\n\t"
    "v_exp_f32 v200, v200\n\t"
    "v_mfma_f32_16x16x32_bf16 v[192:195], a[100:103], a[4:7], v[192:195]\n\t"
    "v_exp_f32 v201, v201\n\t"
    "v_mfma_f32_16x16x32_bf16 v[192:195], a[104:107], a[8:11], v[192:195]\n\t"
    "v_cvt_pk_bf16_f32 v140, v200, v201\n\t"
    "v_mfma_f32_16x16x32_bf16 v[192:195], a[108:111], a[12:15], v[192:195]\n\t"
    "ds_read_b64_tr_b16 a[128:129], v236 offset:32768\n\t"
    "ds_read_b64_tr_b16 a[130:131], v236 offset:36864\n\t"
    "v_mfma_f32_16x16x32_bf16 v[196:199], a[112:115], a[0:3], v[208:211]\n\t"
    "v_exp_f32 v202, v202\n\t"
    "v_mfma_f32_16x16x32_bf16 v[196:199], a[116:119], a[4:7], v[196:199]\n\t"
    "v_exp_f32 v203, v203\n\t"
    "v_mfma_f32_16x16x32_bf16 v[196:199], a[120:123], a[8:11], v[196:199]\n\t"
    "v_cvt_pk_bf16_f32 v141, v202, v203\n\t"
    "v_mfma_f32_16x16x32_bf16 v[196:199], a[124:127], a[12:15], v[196:199]\n\t"
    "s_add_u32 m0, s73, 0x4000\n\t"
    "s_cmp_lt_u32 s98, s96\n\t"
    "v_mfma_f32_16x16x32_bf16 a[192:195], a[208:211], v[176:179], a[192:195]\n\t"
    "v_exp_f32 v204, v204\n\t"
    "v_mfma_f32_16x16x32_bf16 v[16:19], a[144:147], v[160:163], v[16:19]\n\t"
    "v_exp_f32 v205, v205\n\t"
    "v_mfma_f32_16x16x32_bf16 v[16:19], a[176:179], v[176:179], v[16:19]\n\t"
    "v_cvt_pk_bf16_f32 v142, v204, v205\n\t"
    "v_mfma_f32_16x16x32_bf16 v[48:51], a[144:147], v[164:167], v[48:51]\n\t"
    "ds_read_b64_tr_b16 a[160:161], v236 offset:40960\n\t"
    "ds_read_b64_tr_b16 a[162:163], v236 offset:45056\n\t"
    "v_mfma_f32_16x16x32_bf16 v[48:51], a[176:179], v[180:183], v[48:51]\n\t"
    "v_exp_f32 v206, v206\n\t"
    "v_mfma_f32_16x16x32_bf16 v[80:83], a[144:147], v[168:171], v[80:83]\n\t"
    "v_exp_f32 v207, v207\n\t"
    "v_mfma_f32_16x16x32_bf16 v[80:83], a[176:179], v[184:187], v[80:83]\n\t"
    "v_cvt_pk_bf16_f32 v143, v206, v207\n\t"
    "v_mfma_f32_16x16x32_bf16 v[112:115], a[144:147], v[172:175], v[112:115]\n\t"
    "buffer_load_dwordx4 v224, s[28:31], s62 offen nt lds\n\t"
    "v_mfma_f32_16x16x32_bf16 v[112:115], a[176:179], v[188:191], v[112:115]\n\t"
    "ds_read_b128 a[64:67], v232 offset:49152\n\t"
    "ds_read_b128 a[68:71], v233 offset:49152\n\t"
    "v_mfma_f32_16x16x32_bf16 v[200:203], a[96:99], a[16:19], v[212:215]\n\t"
    "v_exp_f32 v192, v192\n\t"
    "v_mfma_f32_16x16x32_bf16 v[200:203], a[100:103], a[20:23], v[200:203]\n\t"
    "v_exp_f32 v193, v193\n\t"
    "v_mfma_f32_16x16x32_bf16 v[200:203], a[104:107], a[24:27], v[200:203]\n\t"
    "v_cvt_pk_bf16_f32 v144, v192, v193\n\t"
    "v_mfma_f32_16x16x32_bf16 v[200:203], a[108:111], a[28:31], v[200:203]\n\t"
    "ds_read_b64_tr_b16 a[132:133], v237 offset:32768\n\t"
    "ds_read_b64_tr_b16 a[134:135], v237 offset:36864\n\t"
    "v_mfma_f32_16x16x32_bf16 v[204:207], a[112:115], a[16:19], v[212:215]\n\t"
    "v_exp_f32 v194, v194\n\t"
    "v_mfma_f32_16x16x32_bf16 v[204:207], a[116:119], a[20:23], v[204:207]\n\t"
    "v_exp_f32 v195, v195\n\t"
    "v_mfma_f32_16x16x32_bf16 v[204:207], a[120:123], a[24:27], v[204:207]\n\t"
    "v_cvt_pk_bf16_f32 v145, v194, v195\n\t"
    "v_mfma_f32_16x16x32_bf16 v[204:207], a[124:127], a[28:31], v[204:207]\n\t"
    "buffer_load_dwordx4 v225, s[28:31], s62 offen offset:1024 nt lds\n\t"
    "v_mfma_f32_16x16x32_bf16 a[196:199], a[208:211], v[180:183], a[196:199]\n\t"
    "v_exp_f32 v196, v196\n\t"
    "v_mfma_f32_16x16x32_bf16 v[20:23], a[148:151], v[160:163], v[20:23]\n\t"
    "v_exp_f32 v197, v197\n\t"
    "v_mfma_f32_16x16x32_bf16 v[20:23], a[180:183], v[176:179], v[20:23]\n\t"
    "v_cvt_pk_bf16_f32 v146, v196, v197\n\t"
    "v_mfma_f32_16x16x32_bf16 v[52:55], a[148:151], v[164:167], v[52:55]\n\t"
    "ds_read_b64_tr_b16 a[164:165], v237 offset:40960\n\t"
    "ds_read_b64_tr_b16 a[166:167], v237 offset:45056\n\t"
    "v_mfma_f32_16x16x32_bf16 v[52:55], a[180:183], v[180:183], v[52:55]\n\t"
    "v_exp_f32 v198, v198\n\t"
    "v_mfma_f32_16x16x32_bf16 v[84:87], a[148:151], v[168:171], v[84:87]\n\t"
    "v_exp_f32 v199, v199\n\t"
    "v_mfma_f32_16x16x32_bf16 v[84:87], a[180:183], v[184:187], v[84:87]\n\t"
    "v_cvt_pk_bf16_f32 v147, v198, v199\n\t"
    "v_mfma_f32_16x16x32_bf16 v[116:119], a[148:151], v[172:175], v[116:119]\n\t"
    "s_cbranch_scc1 .Lpad5_%=\n\t"
    ".Lpadb5_%=:\n\t"
    "s_max_u32 s97, s97, 134\n\t"
    "v_mfma_f32_16x16x32_bf16 v[116:119], a[180:183], v[188:191], v[116:119]\n\t"
    "ds_read_b128 a[72:75], v234 offset:49152\n\t"
    "ds_read_b128 a[76:79], v235 offset:49152\n\t"
    "v_mfma_f32_16x16x32_bf16 v[192:195], a[96:99], a[32:35], v[216:219]\n\t"
    "v_exp_f32 v200, v200\n\t"
    "v_mfma_f32_16x16x32_bf16 v[192:195], a[100:103], a[36:39], v[192:195]\n\t"
    "v_exp_f32 v201, v201\n\t"
    "v_mfma_f32_16x16x32_bf16 v[192:195], a[104:107], a[40:43], v[192:195]\n\t"
    "v_cvt_pk_bf16_f32 v148, v200, v201\n\t"
    "v_mfma_f32_16x16x32_bf16 v[192:195], a[108:111], a[44:47], v[192:195]\n\t"
    "ds_read_b64_tr_b16 a[136:137], v238 offset:32768\n\t"
    "ds_read_b64_tr_b16 a[138:139], v238 offset:36864\n\t"
    "v_mfma_f32_16x16x32_bf16 v[196:199], a[112:115], a[32:35], v[216:219]\n\t"
    "v_exp_f32 v202, v202\n\t"
    "v_mfma_f32_16x16x32_bf16 v[196:199], a[116:119], a[36:39], v[196:199]\n\t"
    "v_exp_f32 v203, v203\n\t"
    "v_mfma_f32_16x16x32_bf16 v[196:199], a[120:123], a[40:43], v[196:199]\n\t"
    "v_cvt_pk_bf16_f32 v149, v202, v203\n\t"
    "v_mfma_f32_16x16x32_bf16 v[196:199], a[124:127], a[44:47], v[196:199]\n\t"
    "buffer_load_dwordx4 v226, s[28:31], s62 offen offset:2048 nt lds\n\t"
    "v_mfma_f32_16x16x32_bf16 a[200:203], a[208:211], v[184:187], a[200:203]\n\t"
    "v_exp_f32 v204, v204\n\t"
    "v_mfma_f32_16x16x32_bf16 v[24:27], a[152:155], v[160:163], v[24:27]\n\t"
    "v_exp_f32 v205, v205\n\t"
    "v_mfma_f32_16x16x32_bf16 v[24:27], a[184:187], v[176:179], v[24:27]\n\t"
    "v_cvt_pk_bf16_f32 v150, v204, v205\n\t"
    "v_mfma_f32_16x16x32_bf16 v[56:59], a[152:155], v[164:167], v[56:59]\n\t"
    "ds_read_b64_tr_b16 a[168:169], v238 offset:40960\n\t"
    "ds_read_b64_tr_b16 a[170:171], v238 offset:45056\n\t"
    "v_mfma_f32_16x16x32_bf16 v[56:59], a[184:187], v[180:183], v[56:59]\n\t"
    "v_exp_f32 v206, v206\n\t"
    "v_mfma_f32_16x16x32_bf16 v[88:91], a[152:155], v[168:171], v[88:91]\n\t"
    "v_exp_f32 v207, v207\n\t"
    "v_mfma_f32_16x16x32_bf16 v[88:91], a[184:187], v[184:187], v[88:91]\n\t"
    "v_cvt_pk_bf16_f32 v151, v206, v207\n\t"
    "v_mfma_f32_16x16x32_bf16 v[120:123], a[152:155], v[172:175], v[120:123]\n\t"
    "buffer_load_dwordx4 v227, s[28:31], s62 offen offset:3072 nt lds\n\t"
    "v_mfma_f32_16x16x32_bf16 v[120:123], a[184:187], v[188:191], v[120:123]\n\t"
    "ds_read_b128 a[80:83], v232 offset:53248\n\t"
    "ds_read_b128 a[84:87], v233 offset:53248\n\t"
    "v_mfma_f32_16x16x32_bf16 v[200:203], a[96:99], a[48:51], v[220:223]\n\t"
    "v_exp_f32 v192, v192\n\t"
    "v_mfma_f32_16x16x32_bf16 v[200:203], a[100:103], a[52:55], v[200:203]\n\t"
    "v_exp_f32 v193, v193\n\t"
    "v_mfma_f32_16x16x32_bf16 v[200:203], a[104:107], a[56:59], v[200:203]\n\t"
    "v_cvt_pk_bf16_f32 v152, v192, v193\n\t"
    "v_mfma_f32_16x16x32_bf16 v[200:203], a[108:111], a[60:63], v[200:203]\n\t"
    "ds_read_b64_tr_b16 a[140:141], v239 offset:32768\n\t"
    "ds_read_b64_tr_b16 a[142:143], v239 offset:36864\n\t"
    "v_mfma_f32_16x16x32_bf16 v[204:207], a[112:115], a[48:51], v[220:223]\n\t"
    "v_exp_f32 v194, v194\n\t"
    "v_mfma_f32_16x16x32_bf16 v[204:207], a[116:119], a[52:55], v[204:207]\n\t"
    "v_exp_f32 v195, v195\n\t"
    "v_mfma_f32_16x16x32_bf16 v[204:207], a[120:123], a[56:59], v[204:207]\n\t"
    "v_cvt_pk_bf16_f32 v153, v194, v195\n\t"
    "v_mfma_f32_16x16x32_bf16 v[204:207], a[124:127], a[60:63], v[204:207]\n\t"
    "ds_read_b64_tr_b16 a[172:173], v239 offset:40960\n\t"
    "ds_read_b64_tr_b16 a[174:175], v239 offset:45056\n\t"
    "v_mfma_f32_16x16x32_bf16 a[204:207], a[208:211], v[188:191], a[204:207]\n\t"
    "v_exp_f32 v196, v196\n\t"
    "v_mfma_f32_16x16x32_bf16 v[28:31], a[156:159], v[160:163], v[28:31]\n\t"
    "v_exp_f32 v197, v197\n\t"
    "v_mfma_f32_16x16x32_bf16 v[28:31], a[188:191], v[176:179], v[28:31]\n\t"
    "v_cvt_pk_bf16_f32 v154, v196, v197\n\t"
    "v_mfma_f32_16x16x32_bf16 v[60:63], a[156:159], v[164:167], v[60:63]\n\t"
    "ds_read_b128 a[88:91], v234 offset:53248\n\t"
    "ds_read_b128 a[92:95], v235 offset:53248\n\t"
    "v_mfma_f32_16x16x32_bf16 v[60:63], a[188:191], v[180:183], v[60:63]\n\t"
    "v_exp_f32 v198, v198\n\t"
    "v_mfma_f32_16x16x32_bf16 v[92:95], a[156:159], v[168:171], v[92:95]\n\t"
    "v_exp_f32 v199, v199\n\t"
    "v_mfma_f32_16x16x32_bf16 v[92:95], a[188:191], v[184:187], v[92:95]\n\t"
    "v_cvt_pk_bf16_f32 v155, v198, v199\n\t"
    "v_mfma_f32_16x16x32_bf16 v[124:127], a[156:159], v[172:175], v[124:127]\n\t"
    "s_add_u32 s62, s62, 0x4000\n\t"
    "s_sub_u32 s97, s97, 2\n\t"
    "v_mfma_f32_16x16x32_bf16 v[124:127], a[188:191], v[188:191], v[124:127]\n\t"
    "s_waitcnt lgkmcnt(0)\n\t"
    "v_mfma_f32_16x16x32_bf16 v[192:195], a[64:67], a[0:3], v[208:211]\n\t"
    "v_exp_f32 v200, v200\n\t"
    "v_mfma_f32_16x16x32_bf16 v[192:195], a[68:71], a[4:7], v[192:195]\n\t"
    "v_exp_f32 v201, v201\n\t"
    "v_mfma_f32_16x16x32_bf16 v[192:195], a[72:75], a[8:11], v[192:195]\n\t"
    "v_cvt_pk_bf16_f32 v156, v200, v201\n\t"
    "v_mfma_f32_16x16x32_bf16 v[192:195], a[76:79], a[12:15], v[192:195]\n\t"
    "ds_read_b64_tr_b16 a[144:145], v240 offset:32768\n\t"
    "ds_read_b64_tr_b16 a[146:147], v240 offset:36864\n\t"
    "v_mfma_f32_16x16x32_bf16 v[196:199], a[80:83], a[0:3], v[208:211]\n\t"
    "v_exp_f32 v202, v202\n\t"
    "v_mfma_f32_16x16x32_bf16 v[196:199], a[84:87], a[4:7], v[196:199]\n\t"
    "v_exp_f32 v203, v203\n\t"
    "v_mfma_f32_16x16x32_bf16 v[196:199], a[88:91], a[8:11], v[196:199]\n\t"
    "v_cvt_pk_bf16_f32 v157, v202, v203\n\t"
    "v_mfma_f32_16x16x32_bf16 v[196:199], a[92:95], a[12:15], v[196:199]\n\t"
    "s_add_u32 m0, s73, 0x10000\n\t"
    "v_mfma_f32_16x16x32_bf16 a[192:195], a[208:211], v[128:131], a[192:195]\n\t"
    "v_exp_f32 v204, v204\n\t"
    "v_mfma_f32_16x16x32_bf16 v[0:3], a[128:131], v[128:131], v[0:3]\n\t"
    "v_exp_f32 v205, v205\n\t"
    "v_mfma_f32_16x16x32_bf16 v[0:3], a[160:163], v[144:147], v[0:3]\n\t"
    "v_cvt_pk_bf16_f32 v158, v204, v205\n\t"
    "v_mfma_f32_16x16x32_bf16 v[32:35], a[128:131], v[132:135], v[32:35]\n\t"
    "ds_read_b64_tr_b16 a[176:177], v240 offset:40960\n\t"
    "ds_read_b64_tr_b16 a[178:179], v240 offset:45056\n\t"
    "v_mfma_f32_16x16x32_bf16 v[32:35], a[160:163], v[148:151], v[32:35]\n\t"
    "v_exp_f32 v206, v206\n\t"
    "v_mfma_f32_16x16x32_bf16 v[64:67], a[128:131], v[136:139], v[64:67]\n\t"
    "v_exp_f32 v207, v207\n\t"
    "v_mfma_f32_16x16x32_bf16 v[64:67], a[160:163], v[152:155], v[64:67]\n\t"
    "v_cvt_pk_bf16_f32 v159, v206, v207\n\t"
    "v_mfma_f32_16x16x32_bf16 v[96:99], a[128:131], v[140:143], v[96:99]\n\t"
    "buffer_load_dwordx4 v228, s[32:35], s60 offen nt lds\n\t"
    "v_mfma_f32_16x16x32_bf16 v[96:99], a[160:163], v[156:159], v[96:99]\n\t"
    "ds_read_b128 a[96:99], v232 offset:57344\n\t"
    "ds_read_b128 a[100:103], v233 offset:57344\n\t"
    "v_mfma_f32_16x16x32_bf16 v[200:203], a[64:67], a[16:19], v[212:215]\n\t"
    "v_exp_f32 v192, v192\n\t"
    "v_mfma_f32_16x16x32_bf16 v[200:203], a[68:71], a[20:23], v[200:203]\n\t"
    "v_exp_f32 v193, v193\n\t"
    "v_mfma_f32_16x16x32_bf16 v[200:203], a[72:75], a[24:27], v[200:203]\n\t"
    "v_cvt_pk_bf16_f32 v160, v192, v193\n\t"
    "v_mfma_f32_16x16x32_bf16 v[200:203], a[76:79], a[28:31], v[200:203]\n\t"
    "ds_read_b64_tr_b16 a[148:149], v241 offset:32768\n\t"
    "ds_read_b64_tr_b16 a[150:151], v241 offset:36864\n\t"
    "v_mfma_f32_16x16x32_bf16 v[204:207], a[80:83], a[16:19], v[212:215]\n\t"
    "v_exp_f32 v194, v194\n\t"
    "v_mfma_f32_16x16x32_bf16 v[204:207], a[84:87], a[20:23], v[204:207]\n\t"
    "v_exp_f32 v195, v195\n\t"
    "v_mfma_f32_16x16x32_bf16 v[204:207], a[88:91], a[24:27], v[204:207]\n\t"
    "v_cvt_pk_bf16_f32 v161, v194, v195\n\t"
    "v_mfma_f32_16x16x32_bf16 v[204:207], a[92:95], a[28:31], v[204:207]\n\t"
    "buffer_load_dwordx4 v229, s[32:35], s60 offen offset:1024 nt lds\n\t"
    "v_mfma_f32_16x16x32_bf16 a[196:199], a[208:211], v[132:135], a[196:199]\n\t"
    "v_exp_f32 v196, v196\n\t"
    "v_mfma_f32_16x16x32_bf16 v[4:7], a[132:135], v[128:131], v[4:7]\n\t"
    "v_exp_f32 v197, v197\n\t"
    "v_mfma_f32_16x16x32_bf16 v[4:7], a[164:167], v[144:147], v[4:7]\n\t"
    "v_cvt_pk_bf16_f32 v162, v196, v197\n\t"
    "v_mfma_f32_16x16x32_bf16 v[36:39], a[132:135], v[132:135], v[36:39]\n\t"
    "ds_read_b64_tr_b16 a[180:181], v241 offset:40960\n\t"
    "ds_read_b64_tr_b16 a[182:183], v241 offset:45056\n\t"
    "v_mfma_f32_16x16x32_bf16 v[36:39], a[164:167], v[148:151], v[36:39]\n\t"
    "v_exp_f32 v198, v198\n\t"
    "v_mfma_f32_16x16x32_bf16 v[68:71], a[132:135], v[136:139], v[68:71]\n\t"
    "v_exp_f32 v199, v199\n\t"
    "v_mfma_f32_16x16x32_bf16 v[68:71], a[164:167], v[152:155], v[68:71]\n\t"
    "v_cvt_pk_bf16_f32 v163, v198, v199\n\t"
    "v_mfma_f32_16x16x32_bf16 v[100:103], a[132:135], v[140:143], v[100:103]\n\t"
    "s_memrealtime s[98:99]\n\t"
    "s_add_u32 s96, s96, s97\n\t"
    "v_mfma_f32_16x16x32_bf16 v[100:103], a[164:167], v[156:159], v[100:103]\n\t"
    "ds_read_b128 a[104:107], v234 offset:57344\n\t"
    "ds_read_b128 a[108:111], v235 offset:57344\n\t"
    "v_mfma_f32_16x16x32_bf16 v[192:195], a[64:67], a[32:35], v[216:219]\n\t"
    "v_exp_f32 v200, v200\n\t"
    "v_mfma_f32_16x16x32_bf16 v[192:195], a[68:71], a[36:39], v[192:195]\n\t"
    "v_exp_f32 v201, v201\n\t"
    "v_mfma_f32_16x16x32_bf16 v[192:195], a[72:75], a[40:43], v[192:195]\n\t"
    "v_cvt_pk_bf16_f32 v164, v200, v201\n\t"
    "v_mfma_f32_16x16x32_bf16 v[192:195], a[76:79], a[44:47], v[192:195]\n\t"
    "ds_read_b64_tr_b16 a[152:153], v242 offset:32768\n\t"
    "ds_read_b64_tr_b16 a[154:155], v242 offset:36864\n\t"
    "v_mfma_f32_16x16x32_bf16 v[196:199], a[80:83], a[32:35], v[216:219]\n\t"
    "v_exp_f32 v202, v202\n\t"
    "v_mfma_f32_16x16x32_bf16 v[196:199], a[84:87], a[36:39], v[196:199]\n\t"
    "v_exp_f32 v203, v203\n\t"
    "v_mfma_f32_16x16x32_bf16 v[196:199], a[88:91], a[40:43], v[196:199]\n\t"
    "v_cvt_pk_bf16_f32 v165, v202, v203\n\t"
    "v_mfma_f32_16x16x32_bf16 v[196:199], a[92:95], a[44:47], v[196:199]\n\t"
    "buffer_load_dwordx4 v228, s[32:35], s60 offen offset:2048 nt lds\n\t"
    "v_mfma_f32_16x16x32_bf16 a[200:203], a[208:211], v[136:139], a[200:203]\n\t"
    "v_exp_f32 v204, v204\n\t"
    "v_mfma_f32_16x16x32_bf16 v[8:11], a[136:139], v[128:131], v[8:11]\n\t"
    "v_exp_f32 v205, v205\n\t"
    "v_mfma_f32_16x16x32_bf16 v[8:11], a[168:171], v[144:147], v[8:11]\n\t"
    "v_cvt_pk_bf16_f32 v166, v204, v205\n\t"
    "v_mfma_f32_16x16x32_bf16 v[40:43], a[136:139], v[132:135], v[40:43]\n\t"
    "ds_read_b64_tr_b16 a[184:185], v242 offset:40960\n\t"
    "ds_read_b64_tr_b16 a[186:187], v242 offset:45056\n\t"
    "v_mfma_f32_16x16x32_bf16 v[40:43], a[168:171], v[148:151], v[40:43]\n\t"
    "v_exp_f32 v206, v206\n\t"
    "v_mfma_f32_16x16x32_bf16 v[72:75], a[136:139], v[136:139], v[72:75]\n\t"
    "v_exp_f32 v207, v207\n\t"
    "v_mfma_f32_16x16x32_bf16 v[72:75], a[168:171], v[152:155], v[72:75]\n\t"
    "v_cvt_pk_bf16_f32 v167, v206, v207\n\t"
    "v_mfma_f32_16x16x32_bf16 v[104:107], a[136:139], v[140:143], v[104:107]\n\t"
    "buffer_load_dwordx4 v229, s[32:35], s60 offen offset:3072 nt lds\n\t"
    "v_mfma_f32_16x16x32_bf16 v[104:107], a[168:171], v[156:159], v[104:107]\n\t"
    "ds_read_b128 a[112:115], v232 offset:61440\n\t"
    "ds_read_b128 a[116:119], v233 offset:61440\n\t"
    "v_mfma_f32_16x16x32_bf16 v[200:203], a[64:67], a[48:51], v[220:223]\n\t"
    "v_exp_f32 v192, v192\n\t"
    "v_mfma_f32_16x16x32_bf16 v[200:203], a[68:71], a[52:55], v[200:203]\n\t"
    "v_exp_f32 v193, v193\n\t"
    "v_mfma_f32_16x16x32_bf16 v[200:203], a[72:75], a[56:59], v[200:203]\n\t"
    "v_cvt_pk_bf16_f32 v168, v192, v193\n\t"
    "v_mfma_f32_16x16x32_bf16 v[200:203], a[76:79], a[60:63], v[200:203]\n\t"
    "ds_read_b64_tr_b16 a[156:157], v243 offset:32768\n\t"
    "ds_read_b64_tr_b16 a[158:159], v243 offset:36864\n\t"
    "v_mfma_f32_16x16x32_bf16 v[204:207], a[80:83], a[48:51], v[220:223]\n\t"
    "v_exp_f32 v194, v194\n\t"
    "v_mfma_f32_16x16x32_bf16 v[204:207], a[84:87], a[52:55], v[204:207]\n\t"
    "v_exp_f32 v195, v195\n\t"
    "v_mfma_f32_16x16x32_bf16 v[204:207], a[88:91], a[56:59], v[204:207]\n\t"
    "v_cvt_pk_bf16_f32 v169, v194, v195\n\t"
    "v_mfma_f32_16x16x32_bf16 v[204:207], a[92:95], a[60:63], v[204:207]\n\t"
    "ds_read_b64_tr_b16 a[188:189], v243 offset:40960\n\t"
    "ds_read_b64_tr_b16 a[190:191], v243 offset:45056\n\t"
    "v_mfma_f32_16x16x32_bf16 a[204:207], a[208:211], v[140:143], a[204:207]\n\t"
    "v_exp_f32 v196, v196\n\t"
    "v_mfma_f32_16x16x32_bf16 v[12:15], a[140:143], v[128:131], v[12:15]\n\t"
    "v_exp_f32 v197, v197\n\t"
    "v_mfma_f32_16x16x32_bf16 v[12:15], a[172:175], v[144:147], v[12:15]\n\t"
    "v_cvt_pk_bf16_f32 v170, v196, v197\n\t"
    "v_mfma_f32_16x16x32_bf16 v[44:47], a[140:143], v[132:135], v[44:47]\n\t"
    "ds_read_b128 a[120:123], v234 offset:61440\n\t"
    "ds_read_b128 a[124:127], v235 offset:61440\n\t"
    "v_mfma_f32_16x16x32_bf16 v[44:47], a[172:175], v[148:151], v[44:47]\n\t"
    "v_exp_f32 v198, v198\n\t"
    "v_mfma_f32_16x16x32_bf16 v[76:79], a[140:143], v[136:139], v[76:79]\n\t"
    "v_exp_f32 v199, v199\n\t"
    "v_mfma_f32_16x16x32_bf16 v[76:79], a[172:175], v[152:155], v[76:79]\n\t"
    "v_cvt_pk_bf16_f32 v171, v198, v199\n\t"
    "v_mfma_f32_16x16x32_bf16 v[108:111], a[140:143], v[140:143], v[108:111]\n\t"
    "s_add_u32 s60, s60, 0x4000\n\t"
    "v_mfma_f32_16x16x32_bf16 v[108:111], a[172:175], v[156:159], v[108:111]\n\t"
    "s_waitcnt vmcnt(8) lgkmcnt(0)\n\t"
    "s_barrier\n\t"
    "v_mfma_f32_16x16x32_bf16 v[192:195], a[96:99], a[0:3], v[208:211]\n\t"
    "v_exp_f32 v200, v200\n\t"
    "v_mfma_f32_16x16x32_bf16 v[192:195], a[100:103], a[4:7], v[192:195]\n\t"
    "v_exp_f32 v201, v201\n\t"
    "v_mfma_f32_16x16x32_bf16 v[192:195], a[104:107], a[8:11], v[192:195]\n\t"
    "v_cvt_pk_bf16_f32 v172, v200, v201\n\t"
    "v_mfma_f32_16x16x32_bf16 v[192:195], a[108:111], a[12:15], v[192:195]\n\t"
    "ds_read_b64_tr_b16 a[128:129], v236 offset:49152\n\t"
    "ds_read_b64_tr_b16 a[130:131], v236 offset:53248\n\t"
    "v_mfma_f32_16x16x32_bf16 v[196:199], a[112:115], a[0:3], v[208:211]\n\t"
    "v_exp_f32 v202, v202\n\t"
    "v_mfma_f32_16x16x32_bf16 v[196:199], a[116:119], a[4:7], v[196:199]\n\t"
    "v_exp_f32 v203, v203\n\t"
    "v_mfma_f32_16x16x32_bf16 v[196:199], a[120:123], a[8:11], v[196:199]\n\t"
    "v_cvt_pk_bf16_f32 v173, v202, v203\n\t"
    "v_mfma_f32_16x16x32_bf16 v[196:199], a[124:127], a[12:15], v[196:199]\n\t"
    "s_add_u32 m0, s73, 0x8000\n\t"
    "s_cmp_lt_u32 s98, s96\n\t"
    "v_mfma_f32_16x16x32_bf16 a[192:195], a[208:211], v[144:147], a[192:195]\n\t"
    "v_exp_f32 v204, v204\n\t"
    "v_mfma_f32_16x16x32_bf16 v[16:19], a[144:147], v[128:131], v[16:19]\n\t"
    "v_exp_f32 v205, v205\n\t"
    "v_mfma_f32_16x16x32_bf16 v[16:19], a[176:179], v[144:147], v[16:19]\n\t"
    "v_cvt_pk_bf16_f32 v174, v204, v205\n\t"
    "v_mfma_f32_16x16x32_bf16 v[48:51], a[144:147], v[132:135], v[48:51]\n\t"
    "ds_read_b64_tr_b16 a[160:161], v236 offset:57344\n\t"
    "ds_read_b64_tr_b16 a[162:163], v236 offset:61440\n\t"
    "v_mfma_f32_16x16x32_bf16 v[48:51], a[176:179], v[148:151], v[48:51]\n\t"
    "v_exp_f32 v206, v206\n\t"
    "v_mfma_f32_16x16x32_bf16 v[80:83], a[144:147], v[136:139], v[80:83]\n\t"
    "v_exp_f32 v207, v207\n\t"
    "v_mfma_f32_16x16x32_bf16 v[80:83], a[176:179], v[152:155], v[80:83]\n\t"
    "v_cvt_pk_bf16_f32 v175, v206, v207\n\t"
    "v_mfma_f32_16x16x32_bf16 v[112:115], a[144:147], v[140:143], v[112:115]\n\t"
    "buffer_load_dwordx4 v224, s[28:31], s62 offen nt lds\n\t"
    "v_mfma_f32_16x16x32_bf16 v[112:115], a[176:179], v[156:159], v[112:115]\n\t"
    "ds_read_b128 a[64:67], v232 offset:0\n\t"
    "ds_read_b128 a[68:71], v233 offset:0\n\t"
    "v_mfma_f32_16x16x32_bf16 v[200:203], a[96:99], a[16:19], v[212:215]\n\t"
    "v_exp_f32 v192, v192\n\t"
    "v_mfma_f32_16x16x32_bf16 v[200:203], a[100:103], a[20:23], v[200:203]\n\t"
    "v_exp_f32 v193, v193\n\t"
    "v_mfma_f32_16x16x32_bf16 v[200:203], a[104:107], a[24:27], v[200:203]\n\t"
    "v_cvt_pk_bf16_f32 v176, v192, v193\n\t"
    "v_mfma_f32_16x16x32_bf16 v[200:203], a[108:111], a[28:31], v[200:203]\n\t"
    "ds_read_b64_tr_b16 a[132:133], v237 offset:49152\n\t"
    "ds_read_b64_tr_b16 a[134:135], v237 offset:53248\n\t"
    "v_mfma_f32_16x16x32_bf16 v[204:207], a[112:115], a[16:19], v[212:215]\n\t"
    "v_exp_f32 v194, v194\n\t"
    "v_mfma_f32_16x16x32_bf16 v[204:207], a[116:119], a[20:23], v[204:207]\n\t"
    "v_exp_f32 v195, v195\n\t"
    "v_mfma_f32_16x16x32_bf16 v[204:207], a[120:123], a[24:27], v[204:207]\n\t"
    "v_cvt_pk_bf16_f32 v177, v194, v195\n\t"
    "v_mfma_f32_16x16x32_bf16 v[204:207], a[124:127], a[28:31], v[204:207]\n\t"
    "buffer_load_dwordx4 v225, s[28:31], s62 offen offset:1024 nt lds\n\t"
    "v_mfma_f32_16x16x32_bf16 a[196:199], a[208:211], v[148:151], a[196:199]\n\t"
    "v_exp_f32 v196, v196\n\t"
    "v_mfma_f32_16x16x32_bf16 v[20:23], a[148:151], v[128:131], v[20:23]\n\t"
    "v_exp_f32 v197, v197\n\t"
    "v_mfma_f32_16x16x32_bf16 v[20:23], a[180:183], v[144:147], v[20:23]\n\t"
    "v_cvt_pk_bf16_f32 v178, v196, v197\n\t"
    "v_mfma_f32_16x16x32_bf16 v[52:55], a[148:151], v[132:135], v[52:55]\n\t"
    "ds_read_b64_tr_b16 a[164:165], v237 offset:57344\n\t"
    "ds_read_b64_tr_b16 a[166:167], v237 offset:61440\n\t"
    "v_mfma_f32_16x16x32_bf16 v[52:55], a[180:183], v[148:151], v[52:55]\n\t"
    "v_exp_f32 v198, v198\n\t"
    "v_mfma_f32_16x16x32_bf16 v[84:87], a[148:151], v[136:139], v[84:87]\n\t"
    "v_exp_f32 v199, v199\n\t"
    "v_mfma_f32_16x16x32_bf16 v[84:87], a[180:183], v[152:155], v[84:87]\n\t"
    "v_cvt_pk_bf16_f32 v179, v198, v199\n\t"
    "v_mfma_f32_16x16x32_bf16 v[116:119], a[148:151], v[140:143], v[116:119]\n\t"
    "s_cbranch_scc1 .Lpad7_%=\n\t"
    ".Lpadb7_%=:\n\t"
    "s_max_u32 s97, s97, 134\n\t"
    "v_mfma_f32_16x16x32_bf16 v[116:119], a[180:183], v[156:159], v[116:119]\n\t"
    "ds_read_b128 a[72:75], v234 offset:0\n\t"
    "ds_read_b128 a[76:79], v235 offset:0\n\t"
    "v_mfma_f32_16x16x32_bf16 v[192:195], a[96:99], a[32:35], v[216:219]\n\t"
    "v_exp_f32 v200, v200\n\t"
    "v_mfma_f32_16x16x32_bf16 v[192:195], a[100:103], a[36:39], v[192:195]\n\t"
    "v_exp_f32 v201, v201\n\t"
    "v_mfma_f32_16x16x32_bf16 v[192:195], a[104:107], a[40:43], v[192:195]\n\t"
    "v_cvt_pk_bf16_f32 v180, v200, v201\n\t"
    "v_mfma_f32_16x16x32_bf16 v[192:195], a[108:111], a[44:47], v[192:195]\n\t"
    "ds_read_b64_tr_b16 a[136:137], v238 offset:49152\n\t"
    "ds_read_b64_tr_b16 a[138:139], v238 offset:53248\n\t"
    "v_mfma_f32_16x16x32_bf16 v[196:199], a[112:115], a[32:35], v[216:219]\n\t"
    "v_exp_f32 v202, v202\n\t"
    "v_mfma_f32_16x16x32_bf16 v[196:199], a[116:119], a[36:39], v[196:199]\n\t"
    "v_exp_f32 v203, v203\n\t"
    "v_mfma_f32_16x16x32_bf16 v[196:199], a[120:123], a[40:43], v[196:199]\n\t"
    "v_cvt_pk_bf16_f32 v181, v202, v203\n\t"
    "v_mfma_f32_16x16x32_bf16 v[196:199], a[124:127], a[44:47], v[196:199]\n\t"
    "buffer_load_dwordx4 v226, s[28:31], s62 offen offset:2048 nt lds\n\t"
    "v_mfma_f32_16x16x32_bf16 a[200:203], a[208:211], v[152:155], a[200:203]\n\t"
    "v_exp_f32 v204, v204\n\t"
    "v_mfma_f32_16x16x32_bf16 v[24:27], a[152:155], v[128:131], v[24:27]\n\t"
    "v_exp_f32 v205, v205\n\t"
    "v_mfma_f32_16x16x32_bf16 v[24:27], a[184:187], v[144:147], v[24:27]\n\t"
    "v_cvt_pk_bf16_f32 v182, v204, v205\n\t"
    "v_mfma_f32_16x16x32_bf16 v[56:59], a[152:155], v[132:135], v[56:59]\n\t"
    "ds_read_b64_tr_b16 a[168:169], v238 offset:57344\n\t"
    "ds_read_b64_tr_b16 a[170:171], v238 offset:61440\n\t"
    "v_mfma_f32_16x16x32_bf16 v[56:59], a[184:187], v[148:151], v[56:59]\n\t"
    "v_exp_f32 v206, v206\n\t"
    "v_mfma_f32_16x16x32_bf16 v[88:91], a[152:155], v[136:139], v[88:91]\n\t"
    "v_exp_f32 v207, v207\n\t"
    "v_mfma_f32_16x16x32_bf16 v[88:91], a[184:187], v[152:155], v[88:91]\n\t"
    "v_cvt_pk_bf16_f32 v183, v206, v207\n\t"
    "v_mfma_f32_16x16x32_bf16 v[120:123], a[152:155], v[140:143], v[120:123]\n\t"
    "buffer_load_dwordx4 v227, s[28:31], s62 offen offset:3072 nt lds\n\t"
    "v_mfma_f32_16x16x32_bf16 v[120:123], a[184:187], v[156:159], v[120:123]\n\t"
    "ds_read_b128 a[80:83], v232 offset:4096\n\t"
    "ds_read_b128 a[84:87], v233 offset:4096\n\t"
    "v_mfma_f32_16x16x32_bf16 v[200:203], a[96:99], a[48:51], v[220:223]\n\t"
    "v_exp_f32 v192, v192\n\t"
    "v_mfma_f32_16x16x32_bf16 v[200:203], a[100:103], a[52:55], v[200:203]\n\t"
    "v_exp_f32 v193, v193\n\t"
    "v_mfma_f32_16x16x32_bf16 v[200:203], a[104:107], a[56:59], v[200:203]\n\t"
    "v_cvt_pk_bf16_f32 v184, v192, v193\n\t"
    "v_mfma_f32_16x16x32_bf16 v[200:203], a[108:111], a[60:63], v[200:203]\n\t"
    "ds_read_b64_tr_b16 a[140:141], v239 offset:49152\n\t"
    "ds_read_b64_tr_b16 a[142:143], v239 offset:53248\n\t"
    "v_mfma_f32_16x16x32_bf16 v[204:207], a[112:115], a[48:51], v[220:223]\n\t"
    "v_exp_f32 v194, v194\n\t"
    "v_mfma_f32_16x16x32_bf16 v[204:207], a[116:119], a[52:55], v[204:207]\n\t"
    "v_exp_f32 v195, v195\n\t"
    "v_mfma_f32_16x16x32_bf16 v[204:207], a[120:123], a[56:59], v[204:207]\n\t"
    "v_cvt_pk_bf16_f32 v185, v194, v195\n\t"
    "v_mfma_f32_16x16x32_bf16 v[204:207], a[124:127], a[60:63], v[204:207]\n\t"
    "ds_read_b64_tr_b16 a[172:173], v239 offset:57344\n\t"
    "ds_read_b64_tr_b16 a[174:175], v239 offset:61440\n\t"
    "v_mfma_f32_16x16x32_bf16 a[204:207], a[208:211], v[156:159], a[204:207]\n\t"
    "v_exp_f32 v196, v196\n\t"
    "v_mfma_f32_16x16x32_bf16 v[28:31], a[156:159], v[128:131], v[28:31]\n\t"
    "v_exp_f32 v197, v197\n\t"
    "v_mfma_f32_16x16x32_bf16 v[28:31], a[188:191], v[144:147], v[28:31]\n\t"
    "v_cvt_pk_bf16_f32 v186, v196, v197\n\t"
    "v_mfma_f32_16x16x32_bf16 v[60:63], a[156:159], v[132:135], v[60:63]\n\t"
    "ds_read_b128 a[88:91], v234 offset:4096\n\t"
    "ds_read_b128 a[92:95], v235 offset:4096\n\t"
    "v_mfma_f32_16x16x32_bf16 v[60:63], a[188:191], v[148:151], v[60:63]\n\t"
    "v_exp_f32 v198, v198\n\t"
    "v_mfma_f32_16x16x32_bf16 v[92:95], a[156:159], v[136:139], v[92:95]\n\t"
    "v_exp_f32 v199, v199\n\t"
    "v_mfma_f32_16x16x32_bf16 v[92:95], a[188:191], v[152:155], v[92:95]\n\t"
    "v_cvt_pk_bf16_f32 v187, v198, v199\n\t"
    "v_mfma_f32_16x16x32_bf16 v[124:127], a[156:159], v[140:143], v[124:127]\n\t"
    "s_add_u32 s62, s62, 0x4000\n\t"
    "s_sub_u32 s97, s97, 2\n\t"
    "v_mfma_f32_16x16x32_bf16 v[124:127], a[188:191], v[156:159], v[124:127]\n\t"
    "s_cmp_lg_u32 s60, 0x44000\n\t"
    "s_waitcnt lgkmcnt(0)\n\t"
    "s_cbranch_scc1 .Lloop%=\n\t"
    ".Ldrain%=:\n\t"
    "v_mfma_f32_16x16x32_bf16 v[0:3], a[128:131], v[160:163], v[0:3]\n\t"
    "ds_read_b64_tr_b16 a[144:145], v240 offset:49152\n\t"
    "ds_read_b64_tr_b16 a[146:147], v240 offset:53248\n\t"
    "v_mfma_f32_16x16x32_bf16 v[0:3], a[160:163], v[176:179], v[0:3]\n\t"
    "ds_read_b64_tr_b16 a[176:177], v240 offset:57344\n\t"
    "ds_read_b64_tr_b16 a[178:179], v240 offset:61440\n\t"
    "v_mfma_f32_16x16x32_bf16 v[4:7], a[132:135], v[160:163], v[4:7]\n\t"
    "ds_read_b64_tr_b16 a[148:149], v241 offset:49152\n\t"
    "ds_read_b64_tr_b16 a[150:151], v241 offset:53248\n\t"
    "v_mfma_f32_16x16x32_bf16 v[4:7], a[164:167], v[176:179], v[4:7]\n\t"
    "ds_read_b64_tr_b16 a[180:181], v241 offset:57344\n\t"
    "ds_read_b64_tr_b16 a[182:183], v241 offset:61440\n\t"
    "v_mfma_f32_16x16x32_bf16 v[8:11], a[136:139], v[160:163], v[8:11]\n\t"
    "ds_read_b64_tr_b16 a[152:153], v242 offset:49152\n\t"
    "ds_read_b64_tr_b16 a[154:155], v242 offset:53248\n\t"
    "v_mfma_f32_16x16x32_bf16 v[8:11], a[168:171], v[176:179], v[8:11]\n\t"
    "ds_read_b64_tr_b16 a[184:185], v242 offset:57344\n\t"
    "ds_read_b64_tr_b16 a[186:187], v242 offset:61440\n\t"
    "v_mfma_f32_16x16x32_bf16 v[12:15], a[140:143], v[160:163], v[12:15]\n\t"
    "ds_read_b64_tr_b16 a[156:157], v243 offset:49152\n\t"
    "ds_read_b64_tr_b16 a[158:159], v243 offset:53248\n\t"
    "v_mfma_f32_16x16x32_bf16 v[12:15], a[172:175], v[176:179], v[12:15]\n\t"
    "ds_read_b64_tr_b16 a[188:189], v243 offset:57344\n\t"
    "ds_read_b64_tr_b16 a[190:191], v243 offset:61440\n\t"
    "v_mfma_f32_16x16x32_bf16 a[192:195], a[208:211], v[160:163], a[192:195]\n\t"
    "v_exp_f32 v200, v200\n\t"
    "v_mfma_f32_16x16x32_bf16 a[192:195], a[208:211], v[176:179], a[192:195]\n\t"
    "v_exp_f32 v201, v201\n\t"
    "v_mfma_f32_16x16x32_bf16 a[196:199], a[208:211], v[164:167], a[196:199]\n\t"
    "v_exp_f32 v202, v202\n\t"
    "v_mfma_f32_16x16x32_bf16 a[196:199], a[208:211], v[180:183], a[196:199]\n\t"
    "v_exp_f32 v203, v203\n\t"
    "v_mfma_f32_16x16x32_bf16 a[200:203], a[208:211], v[168:171], a[200:203]\n\t"
    "v_exp_f32 v204, v204\n\t"
    "v_mfma_f32_16x16x32_bf16 a[200:203], a[208:211], v[184:187], a[200:203]\n\t"
    "v_exp_f32 v205, v205\n\t"
    "s_waitcnt lgkmcnt(0)\n\t"
    "v_mfma_f32_16x16x32_bf16 v[16:19], a[144:147], v[160:163], v[16:19]\n\t"
    "v_exp_f32 v206, v206\n\t"
    "v_mfma_f32_16x16x32_bf16 v[16:19], a[176:179], v[176:179], v[16:19]\n\t"
    "v_exp_f32 v207, v207\n\t"
    "v_mfma_f32_16x16x32_bf16 v[20:23], a[148:151], v[160:163], v[20:23]\n\t"
    "v_cvt_pk_bf16_f32 v188, v200, v201\n\t"
    "v_mfma_f32_16x16x32_bf16 v[20:23], a[180:183], v[176:179], v[20:23]\n\t"
    "v_cvt_pk_bf16_f32 v189, v202, v203\n\t"
    "v_cvt_pk_bf16_f32 v190, v204, v205\n\t"
    "v_mfma_f32_16x16x32_bf16 v[24:27], a[152:155], v[160:163], v[24:27]\n\t"
    "v_cvt_pk_bf16_f32 v191, v206, v207\n\t"
    "v_mfma_f32_16x16x32_bf16 v[24:27], a[184:187], v[176:179], v[24:27]\n\t"
    "v_and_b32 v196, 7, v231\n\t"
    "v_mfma_f32_16x16x32_bf16 v[28:31], a[156:159], v[160:163], v[28:31]\n\t"
    "v_and_b32 v249, 0xffffffe0, v246\n\t"
    "v_mfma_f32_16x16x32_bf16 v[28:31], a[188:191], v[176:179], v[28:31]\n\t"
    "v_lshl_or_b32 v196, v196, 2, v249\n\t"
    "buffer_load_dword v254, v196, s[56:59], 0 offen offset:128 sc1\n\t"
    "v_mfma_f32_16x16x32_bf16 a[204:207], a[208:211], v[172:175], a[204:207]\n\t"
    "s_lshl_b32 s64, s70, 9\n\t"
    "v_mfma_f32_16x16x32_bf16 a[204:207], a[208:211], v[188:191], a[204:207]\n\t"
    "s_add_u32 s64, s64, 0x20800\n\t"
    "v_mfma_f32_16x16x32_bf16 v[32:35], a[128:131], v[164:167], v[32:35]\n\t"
    "v_lshl_add_u32 v146, v231, 3, s64\n\t"
    "s_mov_b32 s60, 0x4e800000\n\t"
    "v_mfma_f32_16x16x32_bf16 v[32:35], a[160:163], v[180:183], v[32:35]\n\t"
    "v_cvt_pk_bf16_f32 v128, v0, v1\n\t"
    "v_mfma_f32_16x16x32_bf16 v[36:39], a[132:135], v[164:167], v[36:39]\n\t"
    "v_cvt_pk_bf16_f32 v129, v2, v3\n\t"
    "v_mfma_f32_16x16x32_bf16 v[36:39], a[164:167], v[180:183], v[36:39]\n\t"
    "v_cvt_pk_bf16_f32 v130, v4, v5\n\t"
    "v_cvt_pk_bf16_f32 v131, v6, v7\n\t"
    "v_mfma_f32_16x16x32_bf16 v[40:43], a[136:139], v[164:167], v[40:43]\n\t"
    "buffer_store_dwordx4 v[128:131], v244, s[48:51], s80 offen offset:0 sc1\n\t"
    "v_mfma_f32_16x16x32_bf16 v[40:43], a[168:171], v[180:183], v[40:43]\n\t"
    "v_cvt_pk_bf16_f32 v132, v8, v9\n\t"
    "v_cvt_pk_bf16_f32 v133, v10, v11\n\t"
    "v_mfma_f32_16x16x32_bf16 v[44:47], a[140:143], v[164:167], v[44:47]\n\t"
    "v_cvt_pk_bf16_f32 v134, v12, v13\n\t"
    "v_mfma_f32_16x16x32_bf16 v[44:47], a[172:175], v[180:183], v[44:47]\n\t"
    "v_cvt_pk_bf16_f32 v135, v14, v15\n\t"
    "v_mfma_f32_16x16x32_bf16 v[48:51], a[144:147], v[164:167], v[48:51]\n\t"
    "buffer_store_dwordx4 v[132:135], v244, s[48:51], s80 offen offset:1024 sc1\n\t"
    "v_cvt_pk_bf16_f32 v136, v16, v17\n\t"
    "v_mfma_f32_16x16x32_bf16 v[48:51], a[176:179], v[180:183], v[48:51]\n\t"
    "v_cvt_pk_bf16_f32 v137, v18, v19\n\t"
    "v_mfma_f32_16x16x32_bf16 v[52:55], a[148:151], v[164:167], v[52:55]\n\t"
    "v_cvt_pk_bf16_f32 v138, v20, v21\n\t"
    "v_cvt_pk_bf16_f32 v139, v22, v23\n\t"
    "v_mfma_f32_16x16x32_bf16 v[52:55], a[180:183], v[180:183], v[52:55]\n\t"
    "buffer_store_dwordx4 v[136:139], v244, s[48:51], s80 offen offset:2048 sc1\n\t"
    "v_mfma_f32_16x16x32_bf16 v[56:59], a[152:155], v[164:167], v[56:59]\n\t"
    "v_cvt_pk_bf16_f32 v140, v24, v25\n\t"
    "v_mfma_f32_16x16x32_bf16 v[56:59], a[184:187], v[180:183], v[56:59]\n\t"
    "v_cvt_pk_bf16_f32 v141, v26, v27\n\t"
    "v_cvt_pk_bf16_f32 v142, v28, v29\n\t"
    "v_mfma_f32_16x16x32_bf16 v[60:63], a[156:159], v[164:167], v[60:63]\n\t"
    "v_cvt_pk_bf16_f32 v143, v30, v31\n\t"
    "v_mfma_f32_16x16x32_bf16 v[60:63], a[188:191], v[180:183], v[60:63]\n\t"
    "buffer_store_dwordx4 v[140:143], v244, s[48:51], s80 offen offset:3072 sc1\n\t"
    "s_bitcmp1_b32 s80, 30\n\t"
    "s_cbranch_scc1 .Lown0_%=\n\t"
    ".Lownb0_%=:\n\t"
    "v_mfma_f32_16x16x32_bf16 v[64:67], a[128:131], v[168:171], v[64:67]\n\t"
    "v_mul_f32 v144, s53, v208\n\t"
    "v_accvgpr_read_b32 v145, a192\n\t"
    "s_lshl_b64 exec, -1, 16\n\t"
    "v_mul_f32 v144, s53, v212\n\t"
    "v_accvgpr_read_b32 v145, a196\n\t"
    "s_lshl_b64 exec, -1, 32\n\t"
    "v_mul_f32 v144, s53, v216\n\t"
    "v_accvgpr_read_b32 v145, a200\n\t"
    "s_lshl_b64 exec, -1, 48\n\t"
    "v_mul_f32 v144, s53, v220\n\t"
    "v_accvgpr_read_b32 v145, a204\n\t"
    "s_mov_b64 exec, -1\n\t"
    "v_mfma_f32_16x16x32_bf16 v[64:67], a[160:163], v[184:187], v[64:67]\n\t"
    "buffer_store_dwordx2 v[144:145], v245, s[48:51], 0 offen sc1\n\t"
    "ds_write_b64 v146, v[144:145]\n\t"
    "v_mfma_f32_16x16x32_bf16 v[68:71], a[132:135], v[168:171], v[68:71]\n\t"
    "v_cvt_pk_bf16_f32 v128, v32, v33\n\t"
    "v_mfma_f32_16x16x32_bf16 v[68:71], a[164:167], v[184:187], v[68:71]\n\t"
    "v_cvt_pk_bf16_f32 v129, v34, v35\n\t"
    "v_cvt_pk_bf16_f32 v130, v36, v37\n\t"
    "v_mfma_f32_16x16x32_bf16 v[72:75], a[136:139], v[168:171], v[72:75]\n\t"
    "v_cvt_pk_bf16_f32 v131, v38, v39\n\t"
    "v_mfma_f32_16x16x32_bf16 v[72:75], a[168:171], v[184:187], v[72:75]\n\t"
    "buffer_store_dwordx4 v[128:131], v244, s[48:51], s81 offen offset:0 sc1\n\t"
    "v_cvt_pk_bf16_f32 v132, v40, v41\n\t"
    "v_mfma_f32_16x16x32_bf16 v[76:79], a[140:143], v[168:171], v[76:79]\n\t"
    "v_cvt_pk_bf16_f32 v133, v42, v43\n\t"
    "v_mfma_f32_16x16x32_bf16 v[76:79], a[172:175], v[184:187], v[76:79]\n\t"
    "v_cvt_pk_bf16_f32 v134, v44, v45\n\t"
    "v_cvt_pk_bf16_f32 v135, v46, v47\n\t"
    "v_mfma_f32_16x16x32_bf16 v[80:83], a[144:147], v[168:171], v[80:83]\n\t"
    "buffer_store_dwordx4 v[132:135], v244, s[48:51], s81 offen offset:1024 sc1\n\t"
    "v_mfma_f32_16x16x32_bf16 v[80:83], a[176:179], v[184:187], v[80:83]\n\t"
    "v_cvt_pk_bf16_f32 v136, v48, v49\n\t"
    "v_cvt_pk_bf16_f32 v137, v50, v51\n\t"
    "v_mfma_f32_16x16x32_bf16 v[84:87], a[148:151], v[168:171], v[84:87]\n\t"
    "v_cvt_pk_bf16_f32 v138, v52, v53\n\t"
    "v_mfma_f32_16x16x32_bf16 v[84:87], a[180:183], v[184:187], v[84:87]\n\t"
    "v_cvt_pk_bf16_f32 v139, v54, v55\n\t"
    "buffer_store_dwordx4 v[136:139], v244, s[48:51], s81 offen offset:2048 sc1\n\t"
    "v_mfma_f32_16x16x32_bf16 v[88:91], a[152:155], v[168:171], v[88:91]\n\t"
    "v_cvt_pk_bf16_f32 v140, v56, v57\n\t"
    "v_mfma_f32_16x16x32_bf16 v[88:91], a[184:187], v[184:187], v[88:91]\n\t"
    "v_cvt_pk_bf16_f32 v141, v58, v59\n\t"
    "v_cvt_pk_bf16_f32 v142, v60, v61\n\t"
    "v_mfma_f32_16x16x32_bf16 v[92:95], a[156:159], v[168:171], v[92:95]\n\t"
    "v_cvt_pk_bf16_f32 v143, v62, v63\n\t"
    "v_mfma_f32_16x16x32_bf16 v[92:95], a[188:191], v[184:187], v[92:95]\n\t"
    "buffer_store_dwordx4 v[140:143], v244, s[48:51], s81 offen offset:3072 sc1\n\t"
    "s_bitcmp1_b32 s81, 30\n\t"
    "s_cbranch_scc1 .Lown1_%=\n\t"
    ".Lownb1_%=:\n\t"
    "v_mfma_f32_16x16x32_bf16 v[96:99], a[128:131], v[172:175], v[96:99]\n\t"
    "v_lshlrev_b32 v146, 1, v244\n\t"
    "v_mfma_f32_16x16x32_bf16 v[96:99], a[160:163], v[188:191], v[96:99]\n\t"
    "v_add_u32 v146, 0x20800, v146\n\t"
    "s_waitcnt lgkmcnt(0)\n\t"
    "s_barrier\n\t"
    "v_mfma_f32_16x16x32_bf16 v[100:103], a[132:135], v[172:175], v[100:103]\n\t"
    "ds_read_b128 v[150:153], v146\n\t"
    "ds_read_b128 v[154:157], v146 offset:16\n\t"
    "v_mfma_f32_16x16x32_bf16 v[100:103], a[164:167], v[188:191], v[100:103]\n\t"
    "v_cvt_pk_bf16_f32 v128, v64, v65\n\t"
    "v_mfma_f32_16x16x32_bf16 v[104:107], a[136:139], v[172:175], v[104:107]\n\t"
    "v_cvt_pk_bf16_f32 v129, v66, v67\n\t"
    "v_cvt_pk_bf16_f32 v130, v68, v69\n\t"
    "v_mfma_f32_16x16x32_bf16 v[104:107], a[168:171], v[188:191], v[104:107]\n\t"
    "v_cvt_pk_bf16_f32 v131, v70, v71\n\t"
    "buffer_store_dwordx4 v[128:131], v244, s[48:51], s82 offen offset:0 sc1\n\t"
    "v_mfma_f32_16x16x32_bf16 v[108:111], a[140:143], v[172:175], v[108:111]\n\t"
    "v_cvt_pk_bf16_f32 v132, v72, v73\n\t"
    "v_mfma_f32_16x16x32_bf16 v[108:111], a[172:175], v[188:191], v[108:111]\n\t"
    "v_cvt_pk_bf16_f32 v133, v74, v75\n\t"
    "v_cvt_pk_bf16_f32 v134, v76, v77\n\t"
    "v_mfma_f32_16x16x32_bf16 v[112:115], a[144:147], v[172:175], v[112:115]\n\t"
    "v_cvt_pk_bf16_f32 v135, v78, v79\n\t"
    "buffer_store_dwordx4 v[132:135], v244, s[48:51], s82 offen offset:1024 sc1\n\t"
    "v_mfma_f32_16x16x32_bf16 v[112:115], a[176:179], v[188:191], v[112:115]\n\t"
    "v_cvt_pk_bf16_f32 v136, v80, v81\n\t"
    "v_mfma_f32_16x16x32_bf16 v[116:119], a[148:151], v[172:175], v[116:119]\n\t"
    "v_cvt_pk_bf16_f32 v137, v82, v83\n\t"
    "v_cvt_pk_bf16_f32 v138, v84, v85\n\t"
    "v_mfma_f32_16x16x32_bf16 v[116:119], a[180:183], v[188:191], v[116:119]\n\t"
    "v_cvt_pk_bf16_f32 v139, v86, v87\n\t"
    "buffer_store_dwordx4 v[136:139], v244, s[48:51], s82 offen offset:2048 sc1\n\t"
    "v_mfma_f32_16x16x32_bf16 v[120:123], a[152:155], v[172:175], v[120:123]\n\t"
    "v_cvt_pk_bf16_f32 v140, v88, v89\n\t"
    "v_mfma_f32_16x16x32_bf16 v[120:123], a[184:187], v[188:191], v[120:123]\n\t"
    "v_cvt_pk_bf16_f32 v141, v90, v91\n\t"
    "v_cvt_pk_bf16_f32 v142, v92, v93\n\t"
    "v_mfma_f32_16x16x32_bf16 v[124:127], a[156:159], v[172:175], v[124:127]\n\t"
    "v_cvt_pk_bf16_f32 v143, v94, v95\n\t"
    "buffer_store_dwordx4 v[140:143], v244, s[48:51], s82 offen offset:3072 sc1\n\t"
    "v_mfma_f32_16x16x32_bf16 v[124:127], a[188:191], v[188:191], v[124:127]\n\t"
    "s_waitcnt lgkmcnt(0)\n\t"
    "v_max3_f32 v248, v151, v153, v155\n\t"
    "v_max_f32 v248, v248, v157\n\t"
    "v_cmp_lt_f32 vcc, s60, v248\n\t"
    "s_bitcmp1_b32 s82, 30\n\t"
    "s_cbranch_scc1 .Lown2_%=\n\t"
    ".Lownb2_%=:\n\t"
    "s_cbranch_vccnz .Lrestart%=\n\t"
    ".Lnorst%=:\n\t"
    "s_waitcnt vmcnt(8)\n\t"
    "s_mov_b32 exec_hi, 0\n\t"
    "s_mov_b32 exec_lo, s84\n\t"
    "buffer_store_dword v254, v247, s[56:59], 0 offen sc1\n\t"
    "s_mov_b64 exec, -1\n\t"
    "v_cvt_pk_bf16_f32 v128, v96, v97\n\t"
    "v_cvt_pk_bf16_f32 v129, v98, v99\n\t"
    "v_cvt_pk_bf16_f32 v130, v100, v101\n\t"
    "v_cvt_pk_bf16_f32 v131, v102, v103\n\t"
    "buffer_store_dwordx4 v[128:131], v244, s[48:51], s83 offen offset:0 sc1\n\t"
    "v_cvt_pk_bf16_f32 v132, v104, v105\n\t"
    "v_cvt_pk_bf16_f32 v133, v106, v107\n\t"
    "v_cvt_pk_bf16_f32 v134, v108, v109\n\t"
    "v_cvt_pk_bf16_f32 v135, v110, v111\n\t"
    "buffer_store_dwordx4 v[132:135], v244, s[48:51], s83 offen offset:1024 sc1\n\t"
    "v_cvt_pk_bf16_f32 v136, v112, v113\n\t"
    "v_cvt_pk_bf16_f32 v137, v114, v115\n\t"
    "v_cvt_pk_bf16_f32 v138, v116, v117\n\t"
    "v_cvt_pk_bf16_f32 v139, v118, v119\n\t"
    "buffer_store_dwordx4 v[136:139], v244, s[48:51], s83 offen offset:2048 sc1\n\t"
    "v_cvt_pk_bf16_f32 v140, v120, v121\n\t"
    "v_cvt_pk_bf16_f32 v141, v122, v123\n\t"
    "v_cvt_pk_bf16_f32 v142, v124, v125\n\t"
    "v_cvt_pk_bf16_f32 v143, v126, v127\n\t"
    "buffer_store_dwordx4 v[140:143], v244, s[48:51], s83 offen offset:3072 sc1\n\t"
    "s_bitcmp1_b32 s83, 30\n\t"
    "s_cbranch_scc1 .Lown3_%=\n\t"
    ".Lownb3_%=:\n\t"
    "s_waitcnt vmcnt(9)\n\t"
    "s_mov_b32 exec_hi, 0\n\t"
    "s_mov_b32 exec_lo, s85\n\t"
    "buffer_store_dword v254, v247, s[56:59], 0 offen sc1\n\t"
    "s_mov_b64 exec, -1\n\t"
    "v_and_b32 v249, 15, v231\n\t"
    "s_sub_u32 s64, s52, s72\n\t"
    "s_and_b32 s64, s64, 3\n\t"
    "s_lshl_b32 s64, s64, 7\n\t"
    "s_lshr_b32 s65, s72, 1\n\t"
    "s_lshl_b32 s65, s65, 9\n\t"
    "s_add_u32 s64, s64, s65\n\t"
    "s_add_u32 s64, s64, 0x20800\n\t"
    "v_lshl_add_u32 v146, v249, 3, s64\n\t"
    "ds_read_b64 v[64:65], v146\n\t"
    "v_add_u32 v251, s74, v244\n\t"
    "s_lshr_b32 s64, s70, 1\n\t"
    "s_lshl_b32 s64, s64, 4\n\t"
    "v_add_u32 v249, s64, v249\n\t"
    "v_lshlrev_b32 v250, 8, v249\n\t"
    "v_lshlrev_b32 v249, 3, v249\n\t"
    "v_add_u32 v249, 0x2000, v249\n\t"
    "v_and_b32 v248, 16, v231\n\t"
    "v_lshl_or_b32 v250, v248, 1, v250\n\t"
    "v_lshrrev_b32 v248, 1, v231\n\t"
    "v_and_b32 v248, 16, v248\n\t"
    "s_and_b32 s64, s70, 1\n\t"
    "s_lshl_b32 s64, s64, 7\n\t"
    "v_or3_b32 v250, v250, v248, s64\n\t"
    "s_mov_b32 s60, 0x400000\n\t"
    "v_bfe_u32 v248, v246, 2, 3\n\t"
    "v_mul_u32_u24 v248, 8448, v248\n\t"
    "v_add_u32 v148, 0x21000, v251\n\t"
    "v_readlane_b32 s37, v248, 1\n\t"
    "v_readlane_b32 s38, v248, 2\n\t"
    "v_readlane_b32 s39, v248, 3\n\t"
    "v_readlane_b32 s40, v248, 4\n\t"
    "v_readlane_b32 s41, v248, 5\n\t"
    "v_readlane_b32 s42, v248, 6\n\t"
    "v_readlane_b32 s43, v248, 7\n\t"
    "s_waitcnt lgkmcnt(0)\n\t"
    "s_barrier\n\t"
    "ds_read_b128 v[0:3], v148\n\t"
    "ds_read_b128 v[4:7], v148 offset:1024\n\t"
    "s_waitcnt vmcnt(6)\n\t"
    "s_mov_b32 exec_hi, 0\n\t"
    "s_mov_b32 exec_lo, s86\n\t"
    "buffer_store_dword v254, v247, s[56:59], 0 offen sc1\n\t"
    "s_mov_b32 exec_lo, 0xff\n\t"
    "buffer_load_dword v248, v246, s[56:59], 0 offen sc1\n\t"
    "s_lshl_b32 s65, s71, 3\n\t"
    "s_add_u32 s65, s65, s72\n\t"
    "s_mul_i32 s64, s65, 67584\n\t"
    "s_add_u32 s24, s20, s64\n\t"
    "s_addc_u32 s25, s21, 0\n\t"
    "s_and_b32 s25, s25, 0xffff\n\t"
    "s_mov_b32 s26, 67584\n\t"
    "s_mov_b32 s27, 0x20000\n\t"
    "s_lshl_b32 s64, s65, 13\n\t"
    "s_add_u32 s32, s22, s64\n\t"
    "s_addc_u32 s33, s23, 0\n\t"
    "s_and_b32 s33, s33, 0xffff\n\t"
    "s_mov_b32 s34, 0x2000\n\t"
    "s_mov_b32 s35, 0x20000\n\t"
    "s_waitcnt vmcnt(3)\n\t"
    "s_mov_b32 exec_hi, 0\n\t"
    "s_mov_b32 exec_lo, s87\n\t"
    "buffer_store_dword v254, v247, s[56:59], 0 offen sc1\n\t"
    "s_mov_b64 exec, -1\n\t"
    "s_waitcnt vmcnt(1)\n\t"
    "v_cmp_ne_u32 vcc, v248, v230\n\t"
    ".Lchk0_%=:\n\t"
    "s_and_b32 s64, vcc_lo, 2\n\t"
    "s_cbranch_scc1 .Lpoll0_%=\n\t"
    ".Lgo0_%=:\n\t"
    "buffer_load_dwordx2 v[66:67], v249, s[24:27], s37 offen sc1\n\t"
    "buffer_load_dwordx4 v[8:11], v251, s[24:27], s37 offen offset:0 sc1\n\t"
    "buffer_load_dwordx4 v[12:15], v251, s[24:27], s37 offen offset:1024 sc1\n\t"
    ".Lchk1_%=:\n\t"
    "s_and_b32 s64, vcc_lo, 12\n\t"
    "s_cbranch_scc1 .Lpoll1_%=\n\t"
    ".Lgo1_%=:\n\t"
    "buffer_load_dwordx2 v[68:69], v249, s[24:27], s38 offen sc1\n\t"
    "buffer_load_dwordx2 v[70:71], v249, s[24:27], s39 offen sc1\n\t"
    "buffer_load_dwordx4 v[16:19], v251, s[24:27], s38 offen offset:0 sc1\n\t"
    "buffer_load_dwordx4 v[20:23], v251, s[24:27], s38 offen offset:1024 sc1\n\t"
    "buffer_load_dwordx4 v[24:27], v251, s[24:27], s39 offen offset:0 sc1\n\t"
    "buffer_load_dwordx4 v[28:31], v251, s[24:27], s39 offen offset:1024 sc1\n\t"
    ".Lchk2_%=:\n\t"
    "s_and_b32 s64, vcc_lo, 48\n\t"
    "s_cbranch_scc1 .Lpoll2_%=\n\t"
    ".Lgo2_%=:\n\t"
    "buffer_load_dwordx2 v[72:73], v249, s[24:27], s40 offen sc1\n\t"
    "buffer_load_dwordx2 v[74:75], v249, s[24:27], s41 offen sc1\n\t"
    "buffer_load_dwordx4 v[32:35], v251, s[24:27], s40 offen offset:0 sc1\n\t"
    "buffer_load_dwordx4 v[36:39], v251, s[24:27], s40 offen offset:1024 sc1\n\t"
    "buffer_load_dwordx4 v[40:43], v251, s[24:27], s41 offen offset:0 sc1\n\t"
    "buffer_load_dwordx4 v[44:47], v251, s[24:27], s41 offen offset:1024 sc1\n\t"
    ".Lchk3_%=:\n\t"
    "s_and_b32 s64, vcc_lo, 192\n\t"
    "s_cbranch_scc1 .Lpoll3_%=\n\t"
    ".Lgo3_%=:\n\t"
    "buffer_load_dwordx2 v[76:77], v249, s[24:27], s42 offen sc1\n\t"
    "buffer_load_dwordx2 v[78:79], v249, s[24:27], s43 offen sc1\n\t"
    "buffer_load_dwordx4 v[48:51], v251, s[24:27], s42 offen offset:0 sc1\n\t"
    "buffer_load_dwordx4 v[52:55], v251, s[24:27], s42 offen offset:1024 sc1\n\t"
    "buffer_load_dwordx4 v[56:59], v251, s[24:27], s43 offen offset:0 sc1\n\t"
    "buffer_load_dwordx4 v[60:63], v251, s[24:27], s43 offen offset:1024 sc1\n\t"
    "buffer_load_dword v120, v196, s[56:59], 0 offen offset:128 sc1\n\t"
    "s_waitcnt vmcnt(11) lgkmcnt(0)\n\t"
    "v_min3_f32 v88, v64, v66, v68\n\t"
    "v_min3_f32 v88, v88, v70, v72\n\t"
    "v_min_f32 v88, v88, v74\n\t"
    "v_sub_f32 v80, v88, v64\n\t"
    "v_sub_f32 v81, v88, v66\n\t"
    "v_sub_f32 v82, v88, v68\n\t"
    "v_sub_f32 v83, v88, v70\n\t"
    "v_sub_f32 v84, v88, v72\n\t"
    "v_sub_f32 v85, v88, v74\n\t"
    "v_exp_f32 v80, v80\n\t"
    "v_exp_f32 v81, v81\n\t"
    "v_exp_f32 v82, v82\n\t"
    "v_exp_f32 v83, v83\n\t"
    "v_exp_f32 v84, v84\n\t"
    "v_exp_f32 v85, v85\n\t"
    "v_mul_f32 v89, v80, v65\n\t"
    "v_fmac_f32 v89, v81, v67\n\t"
    "v_fmac_f32 v89, v82, v69\n\t"
    "v_fmac_f32 v89, v83, v71\n\t"
    "v_fmac_f32 v89, v84, v73\n\t"
    "v_fmac_f32 v89, v85, v75\n\t"
    "v_lshlrev_b32 v112, 16, v0\n\t"
    "v_and_b32 v113, 0xffff0000, v0\n\t"
    "v_lshlrev_b32 v114, 16, v1\n\t"
    "v_and_b32 v115, 0xffff0000, v1\n\t"
    "v_lshlrev_b32 v116, 16, v2\n\t"
    "v_and_b32 v117, 0xffff0000, v2\n\t"
    "v_lshlrev_b32 v118, 16, v3\n\t"
    "v_and_b32 v119, 0xffff0000, v3\n\t"
    "v_mul_f32 v96, v80, v112\n\t"
    "v_mul_f32 v97, v80, v113\n\t"
    "v_mul_f32 v98, v80, v114\n\t"
    "v_mul_f32 v99, v80, v115\n\t"
    "v_mul_f32 v100, v80, v116\n\t"
    "v_mul_f32 v101, v80, v117\n\t"
    "v_mul_f32 v102, v80, v118\n\t"
    "v_mul_f32 v103, v80, v119\n\t"
    "v_lshlrev_b32 v112, 16, v4\n\t"
    "v_and_b32 v113, 0xffff0000, v4\n\t"
    "v_lshlrev_b32 v114, 16, v5\n\t"
    "v_and_b32 v115, 0xffff0000, v5\n\t"
    "v_lshlrev_b32 v116, 16, v6\n\t"
    "v_and_b32 v117, 0xffff0000, v6\n\t"
    "v_lshlrev_b32 v118, 16, v7\n\t"
    "v_and_b32 v119, 0xffff0000, v7\n\t"
    "v_mul_f32 v104, v80, v112\n\t"
    "v_mul_f32 v105, v80, v113\n\t"
    "v_mul_f32 v106, v80, v114\n\t"
    "v_mul_f32 v107, v80, v115\n\t"
    "v_mul_f32 v108, v80, v116\n\t"
    "v_mul_f32 v109, v80, v117\n\t"
    "v_mul_f32 v110, v80, v118\n\t"
    "v_mul_f32 v111, v80, v119\n\t"
    "v_lshlrev_b32 v112, 16, v8\n\t"
    "v_and_b32 v113, 0xffff0000, v8\n\t"
    "v_lshlrev_b32 v114, 16, v9\n\t"
    "v_and_b32 v115, 0xffff0000, v9\n\t"
    "v_lshlrev_b32 v116, 16, v10\n\t"
    "v_and_b32 v117, 0xffff0000, v10\n\t"
    "v_lshlrev_b32 v118, 16, v11\n\t"
    "v_and_b32 v119, 0xffff0000, v11\n\t"
    "v_fmac_f32 v96, v81, v112\n\t"
    "v_fmac_f32 v97, v81, v113\n\t"
    "v_fmac_f32 v98, v81, v114\n\t"
    "v_fmac_f32 v99, v81, v115\n\t"
    "v_fmac_f32 v100, v81, v116\n\t"
    "v_fmac_f32 v101, v81, v117\n\t"
    "v_fmac_f32 v102, v81, v118\n\t"
    "v_fmac_f32 v103, v81, v119\n\t"
    "v_lshlrev_b32 v112, 16, v12\n\t"
    "v_and_b32 v113, 0xffff0000, v12\n\t"
    "v_lshlrev_b32 v114, 16, v13\n\t"
    "v_and_b32 v115, 0xffff0000, v13\n\t"
    "v_lshlrev_b32 v116, 16, v14\n\t"
    "v_and_b32 v117, 0xffff0000, v14\n\t"
    "v_lshlrev_b32 v118, 16, v15\n\t"
    "v_and_b32 v119, 0xffff0000, v15\n\t"
    "v_fmac_f32 v104, v81, v112\n\t"
    "v_fmac_f32 v105, v81, v113\n\t"
    "v_fmac_f32 v106, v81, v114\n\t"
    "v_fmac_f32 v107, v81, v115\n\t"
    "v_fmac_f32 v108, v81, v116\n\t"
    "v_fmac_f32 v109, v81, v117\n\t"
    "v_fmac_f32 v110, v81, v118\n\t"
    "v_fmac_f32 v111, v81, v119\n\t"
    "v_lshlrev_b32 v112, 16, v16\n\t"
    "v_and_b32 v113, 0xffff0000, v16\n\t"
    "v_lshlrev_b32 v114, 16, v17\n\t"
    "v_and_b32 v115, 0xffff0000, v17\n\t"
    "v_lshlrev_b32 v116, 16, v18\n\t"
    "v_and_b32 v117, 0xffff0000, v18\n\t"
    "v_lshlrev_b32 v118, 16, v19\n\t"
    "v_and_b32 v119, 0xffff0000, v19\n\t"
    "v_fmac_f32 v96, v82, v112\n\t"
    "v_fmac_f32 v97, v82, v113\n\t"
    "v_fmac_f32 v98, v82, v114\n\t"
    "v_fmac_f32 v99, v82, v115\n\t"
    "v_fmac_f32 v100, v82, v116\n\t"
    "v_fmac_f32 v101, v82, v117\n\t"
    "v_fmac_f32 v102, v82, v118\n\t"
    "v_fmac_f32 v103, v82, v119\n\t"
    "v_lshlrev_b32 v112, 16, v20\n\t"
    "v_and_b32 v113, 0xffff0000, v20\n\t"
    "v_lshlrev_b32 v114, 16, v21\n\t"
    "v_and_b32 v115, 0xffff0000, v21\n\t"
    "v_lshlrev_b32 v116, 16, v22\n\t"
    "v_and_b32 v117, 0xffff0000, v22\n\t"
    "v_lshlrev_b32 v118, 16, v23\n\t"
    "v_and_b32 v119, 0xffff0000, v23\n\t"
    "v_fmac_f32 v104, v82, v112\n\t"
    "v_fmac_f32 v105, v82, v113\n\t"
    "v_fmac_f32 v106, v82, v114\n\t"
    "v_fmac_f32 v107, v82, v115\n\t"
    "v_fmac_f32 v108, v82, v116\n\t"
    "v_fmac_f32 v109, v82, v117\n\t"
    "v_fmac_f32 v110, v82, v118\n\t"
    "v_fmac_f32 v111, v82, v119\n\t"
    "v_lshlrev_b32 v112, 16, v24\n\t"
    "v_and_b32 v113, 0xffff0000, v24\n\t"
    "v_lshlrev_b32 v114, 16, v25\n\t"
    "v_and_b32 v115, 0xffff0000, v25\n\t"
    "v_lshlrev_b32 v116, 16, v26\n\t"
    "v_and_b32 v117, 0xffff0000, v26\n\t"
    "v_lshlrev_b32 v118, 16, v27\n\t"
    "v_and_b32 v119, 0xffff0000, v27\n\t"
    "v_fmac_f32 v96, v83, v112\n\t"
    "v_fmac_f32 v97, v83, v113\n\t"
    "v_fmac_f32 v98, v83, v114\n\t"
    "v_fmac_f32 v99, v83, v115\n\t"
    "v_fmac_f32 v100, v83, v116\n\t"
    "v_fmac_f32 v101, v83, v117\n\t"
    "v_fmac_f32 v102, v83, v118\n\t"
    "v_fmac_f32 v103, v83, v119\n\t"
    "v_lshlrev_b32 v112, 16, v28\n\t"
    "v_and_b32 v113, 0xffff0000, v28\n\t"
    "v_lshlrev_b32 v114, 16, v29\n\t"
    "v_and_b32 v115, 0xffff0000, v29\n\t"
    "v_lshlrev_b32 v116, 16, v30\n\t"
    "v_and_b32 v117, 0xffff0000, v30\n\t"
    "v_lshlrev_b32 v118, 16, v31\n\t"
    "v_and_b32 v119, 0xffff0000, v31\n\t"
    "v_fmac_f32 v104, v83, v112\n\t"
    "v_fmac_f32 v105, v83, v113\n\t"
    "v_fmac_f32 v106, v83, v114\n\t"
    "v_fmac_f32 v107, v83, v115\n\t"
    "v_fmac_f32 v108, v83, v116\n\t"
    "v_fmac_f32 v109, v83, v117\n\t"
    "v_fmac_f32 v110, v83, v118\n\t"
    "v_fmac_f32 v111, v83, v119\n\t"
    "s_waitcnt vmcnt(10)\n\t"
    "v_lshlrev_b32 v112, 16, v32\n\t"
    "v_and_b32 v113, 0xffff0000, v32\n\t"
    "v_lshlrev_b32 v114, 16, v33\n\t"
    "v_and_b32 v115, 0xffff0000, v33\n\t"
    "v_lshlrev_b32 v116, 16, v34\n\t"
    "v_and_b32 v117, 0xffff0000, v34\n\t"
    "v_lshlrev_b32 v118, 16, v35\n\t"
    "v_and_b32 v119, 0xffff0000, v35\n\t"
    "v_fmac_f32 v96, v84, v112\n\t"
    "v_fmac_f32 v97, v84, v113\n\t"
    "v_fmac_f32 v98, v84, v114\n\t"
    "v_fmac_f32 v99, v84, v115\n\t"
    "v_fmac_f32 v100, v84, v116\n\t"
    "v_fmac_f32 v101, v84, v117\n\t"
    "v_fmac_f32 v102, v84, v118\n\t"
    "v_fmac_f32 v103, v84, v119\n\t"
    "s_waitcnt vmcnt(9)\n\t"
    "v_lshlrev_b32 v112, 16, v36\n\t"
    "v_and_b32 v113, 0xffff0000, v36\n\t"
    "v_lshlrev_b32 v114, 16, v37\n\t"
    "v_and_b32 v115, 0xffff0000, v37\n\t"
    "v_lshlrev_b32 v116, 16, v38\n\t"
    "v_and_b32 v117, 0xffff0000, v38\n\t"
    "v_lshlrev_b32 v118, 16, v39\n\t"
    "v_and_b32 v119, 0xffff0000, v39\n\t"
    "v_fmac_f32 v104, v84, v112\n\t"
    "v_fmac_f32 v105, v84, v113\n\t"
    "v_fmac_f32 v106, v84, v114\n\t"
    "v_fmac_f32 v107, v84, v115\n\t"
    "v_fmac_f32 v108, v84, v116\n\t"
    "v_fmac_f32 v109, v84, v117\n\t"
    "v_fmac_f32 v110, v84, v118\n\t"
    "v_fmac_f32 v111, v84, v119\n\t"
    "s_waitcnt vmcnt(8)\n\t"
    "v_lshlrev_b32 v112, 16, v40\n\t"
    "v_and_b32 v113, 0xffff0000, v40\n\t"
    "v_lshlrev_b32 v114, 16, v41\n\t"
    "v_and_b32 v115, 0xffff0000, v41\n\t"
    "v_lshlrev_b32 v116, 16, v42\n\t"
    "v_and_b32 v117, 0xffff0000, v42\n\t"
    "v_lshlrev_b32 v118, 16, v43\n\t"
    "v_and_b32 v119, 0xffff0000, v43\n\t"
    "v_fmac_f32 v96, v85, v112\n\t"
    "v_fmac_f32 v97, v85, v113\n\t"
    "v_fmac_f32 v98, v85, v114\n\t"
    "v_fmac_f32 v99, v85, v115\n\t"
    "v_fmac_f32 v100, v85, v116\n\t"
    "v_fmac_f32 v101, v85, v117\n\t"
    "v_fmac_f32 v102, v85, v118\n\t"
    "v_fmac_f32 v103, v85, v119\n\t"
    "s_waitcnt vmcnt(7)\n\t"
    "v_lshlrev_b32 v112, 16, v44\n\t"
    "v_and_b32 v113, 0xffff0000, v44\n\t"
    "v_lshlrev_b32 v114, 16, v45\n\t"
    "v_and_b32 v115, 0xffff0000, v45\n\t"
    "v_lshlrev_b32 v116, 16, v46\n\t"
    "v_and_b32 v117, 0xffff0000, v46\n\t"
    "v_lshlrev_b32 v118, 16, v47\n\t"
    "v_and_b32 v119, 0xffff0000, v47\n\t"
    "v_fmac_f32 v104, v85, v112\n\t"
    "v_fmac_f32 v105, v85, v113\n\t"
    "v_fmac_f32 v106, v85, v114\n\t"
    "v_fmac_f32 v107, v85, v115\n\t"
    "v_fmac_f32 v108, v85, v116\n\t"
    "v_fmac_f32 v109, v85, v117\n\t"
    "v_fmac_f32 v110, v85, v118\n\t"
    "v_fmac_f32 v111, v85, v119\n\t"
    "s_waitcnt vmcnt(5)\n\t"
    "v_min3_f32 v80, v88, v76, v78\n\t"
    "v_sub_f32 v88, v80, v88\n\t"
    "v_sub_f32 v86, v80, v76\n\t"
    "v_sub_f32 v87, v80, v78\n\t"
    "v_exp_f32 v88, v88\n\t"
    "v_exp_f32 v86, v86\n\t"
    "v_exp_f32 v87, v87\n\t"
    "v_mul_f32 v89, v89, v88\n\t"
    "v_fmac_f32 v89, v86, v77\n\t"
    "v_fmac_f32 v89, v87, v79\n\t"
    "v_rcp_f32 v89, v89\n\t"
    "s_nop 0\n\t"
    "v_mul_f32 v88, v88, v89\n\t"
    "v_mul_f32 v86, v86, v89\n\t"
    "v_mul_f32 v87, v87, v89\n\t"
    "v_mul_f32 v96, v96, v88\n\t"
    "v_mul_f32 v97, v97, v88\n\t"
    "v_mul_f32 v98, v98, v88\n\t"
    "v_mul_f32 v99, v99, v88\n\t"
    "v_mul_f32 v100, v100, v88\n\t"
    "v_mul_f32 v101, v101, v88\n\t"
    "v_mul_f32 v102, v102, v88\n\t"
    "v_mul_f32 v103, v103, v88\n\t"
    "v_mul_f32 v104, v104, v88\n\t"
    "v_mul_f32 v105, v105, v88\n\t"
    "v_mul_f32 v106, v106, v88\n\t"
    "v_mul_f32 v107, v107, v88\n\t"
    "v_mul_f32 v108, v108, v88\n\t"
    "v_mul_f32 v109, v109, v88\n\t"
    "v_mul_f32 v110, v110, v88\n\t"
    "v_mul_f32 v111, v111, v88\n\t"
    "s_waitcnt vmcnt(4)\n\t"
    "v_lshlrev_b32 v112, 16, v48\n\t"
    "v_and_b32 v113, 0xffff0000, v48\n\t"
    "v_lshlrev_b32 v114, 16, v49\n\t"
    "v_and_b32 v115, 0xffff0000, v49\n\t"
    "v_lshlrev_b32 v116, 16, v50\n\t"
    "v_and_b32 v117, 0xffff0000, v50\n\t"
    "v_lshlrev_b32 v118, 16, v51\n\t"
    "v_and_b32 v119, 0xffff0000, v51\n\t"
    "v_fmac_f32 v96, v86, v112\n\t"
    "v_fmac_f32 v97, v86, v113\n\t"
    "v_fmac_f32 v98, v86, v114\n\t"
    "v_fmac_f32 v99, v86, v115\n\t"
    "v_fmac_f32 v100, v86, v116\n\t"
    "v_fmac_f32 v101, v86, v117\n\t"
    "v_fmac_f32 v102, v86, v118\n\t"
    "v_fmac_f32 v103, v86, v119\n\t"
    "s_waitcnt vmcnt(3)\n\t"
    "v_lshlrev_b32 v112, 16, v52\n\t"
    "v_and_b32 v113, 0xffff0000, v52\n\t"
    "v_lshlrev_b32 v114, 16, v53\n\t"
    "v_and_b32 v115, 0xffff0000, v53\n\t"
    "v_lshlrev_b32 v116, 16, v54\n\t"
    "v_and_b32 v117, 0xffff0000, v54\n\t"
    "v_lshlrev_b32 v118, 16, v55\n\t"
    "v_and_b32 v119, 0xffff0000, v55\n\t"
    "v_fmac_f32 v104, v86, v112\n\t"
    "v_fmac_f32 v105, v86, v113\n\t"
    "v_fmac_f32 v106, v86, v114\n\t"
    "v_fmac_f32 v107, v86, v115\n\t"
    "v_fmac_f32 v108, v86, v116\n\t"
    "v_fmac_f32 v109, v86, v117\n\t"
    "v_fmac_f32 v110, v86, v118\n\t"
    "v_fmac_f32 v111, v86, v119\n\t"
    "s_waitcnt vmcnt(2)\n\t"
    "v_lshlrev_b32 v112, 16, v56\n\t"
    "v_and_b32 v113, 0xffff0000, v56\n\t"
    "v_lshlrev_b32 v114, 16, v57\n\t"
    "v_and_b32 v115, 0xffff0000, v57\n\t"
    "v_lshlrev_b32 v116, 16, v58\n\t"
    "v_and_b32 v117, 0xffff0000, v58\n\t"
    "v_lshlrev_b32 v118, 16, v59\n\t"
    "v_and_b32 v119, 0xffff0000, v59\n\t"
    "v_fmac_f32 v96, v87, v112\n\t"
    "v_fmac_f32 v97, v87, v113\n\t"
    "v_fmac_f32 v98, v87, v114\n\t"
    "v_fmac_f32 v99, v87, v115\n\t"
    "v_fmac_f32 v100, v87, v116\n\t"
    "v_fmac_f32 v101, v87, v117\n\t"
    "v_fmac_f32 v102, v87, v118\n\t"
    "v_fmac_f32 v103, v87, v119\n\t"
    "s_waitcnt vmcnt(1)\n\t"
    "v_lshlrev_b32 v112, 16, v60\n\t"
    "v_and_b32 v113, 0xffff0000, v60\n\t"
    "v_lshlrev_b32 v114, 16, v61\n\t"
    "v_and_b32 v115, 0xffff0000, v61\n\t"
    "v_lshlrev_b32 v116, 16, v62\n\t"
    "v_and_b32 v117, 0xffff0000, v62\n\t"
    "v_lshlrev_b32 v118, 16, v63\n\t"
    "v_and_b32 v119, 0xffff0000, v63\n\t"
    "v_fmac_f32 v104, v87, v112\n\t"
    "v_fmac_f32 v105, v87, v113\n\t"
    "v_fmac_f32 v106, v87, v114\n\t"
    "v_fmac_f32 v107, v87, v115\n\t"
    "v_fmac_f32 v108, v87, v116\n\t"
    "v_fmac_f32 v109, v87, v117\n\t"
    "v_fmac_f32 v110, v87, v118\n\t"
    "v_fmac_f32 v111, v87, v119\n\t"
    "v_cvt_pk_bf16_f32 v112, v96, v97\n\t"
    "v_cvt_pk_bf16_f32 v113, v98, v99\n\t"
    "v_cvt_pk_bf16_f32 v114, v100, v101\n\t"
    "v_cvt_pk_bf16_f32 v115, v102, v103\n\t"
    "v_cvt_pk_bf16_f32 v116, v104, v105\n\t"
    "v_cvt_pk_bf16_f32 v117, v106, v107\n\t"
    "v_cvt_pk_bf16_f32 v118, v108, v109\n\t"
    "v_cvt_pk_bf16_f32 v119, v110, v111\n\t"
    "s_nop 1\n\t"
    "v_permlane16_swap_b32 v112, v114\n\t"
    "v_permlane16_swap_b32 v113, v115\n\t"
    "v_permlane16_swap_b32 v116, v118\n\t"
    "v_permlane16_swap_b32 v117, v119\n\t"
    "s_nop 1\n\t"
    "buffer_store_dwordx4 v[112:115], v250, s[32:35], 0 offen offset:0 sc1\n\t"
    "buffer_store_dwordx4 v[116:119], v250, s[32:35], 0 offen offset:64 sc1\n\t"
    "s_waitcnt vmcnt(2)\n\t"
    "v_cmp_ne_u32 vcc, v120, v254\n\t"
    "s_cbranch_vccnz .Lreecho%=\n\t"
    ".Lechoed%=:\n\t"
    "s_endpgm\n\t"
    ".Lown0_%=:\n\t"
    "s_and_b32 s64, s80, 0x1000\n\t"
    "s_add_u32 s64, s64, 0x21000\n\t"
    "v_add_u32 v148, s64, v244\n\t"
    "ds_write_b128 v148, v[128:131] offset:0\n\t"
    "ds_write_b128 v148, v[132:135] offset:1024\n\t"
    "ds_write_b128 v148, v[136:139] offset:2048\n\t"
    "ds_write_b128 v148, v[140:143] offset:3072\n\t"
    "s_branch .Lownb0_%=\n\t"
    ".Lown1_%=:\n\t"
    "s_and_b32 s64, s81, 0x1000\n\t"
    "s_add_u32 s64, s64, 0x21000\n\t"
    "v_add_u32 v148, s64, v244\n\t"
    "ds_write_b128 v148, v[128:131] offset:0\n\t"
    "ds_write_b128 v148, v[132:135] offset:1024\n\t"
    "ds_write_b128 v148, v[136:139] offset:2048\n\t"
    "ds_write_b128 v148, v[140:143] offset:3072\n\t"
    "s_branch .Lownb1_%=\n\t"
    ".Lown2_%=:\n\t"
    "s_and_b32 s64, s82, 0x1000\n\t"
    "s_add_u32 s64, s64, 0x21000\n\t"
    "v_add_u32 v148, s64, v244\n\t"
    "ds_write_b128 v148, v[128:131] offset:0\n\t"
    "ds_write_b128 v148, v[132:135] offset:1024\n\t"
    "ds_write_b128 v148, v[136:139] offset:2048\n\t"
    "ds_write_b128 v148, v[140:143] offset:3072\n\t"
    "s_branch .Lownb2_%=\n\t"
    ".Lown3_%=:\n\t"
    "s_and_b32 s64, s83, 0x1000\n\t"
    "s_add_u32 s64, s64, 0x21000\n\t"
    "v_add_u32 v148, s64, v244\n\t"
    "ds_write_b128 v148, v[128:131] offset:0\n\t"
    "ds_write_b128 v148, v[132:135] offset:1024\n\t"
    "ds_write_b128 v148, v[136:139] offset:2048\n\t"
    "ds_write_b128 v148, v[140:143] offset:3072\n\t"
    "s_branch .Lownb3_%=\n\t"
    ".Lpoll0_%=:\n\t"
    ".Lpl0_%=:\n\t"
    "s_mov_b64 exec, 0xff\n\t"
    "buffer_load_dword v248, v246, s[56:59], 0 offen sc1\n\t"
    "s_mov_b64 exec, -1\n\t"
    "s_waitcnt vmcnt(0)\n\t"
    "v_cmp_ne_u32 vcc, v248, v230\n\t"
    "s_and_b32 s64, vcc_lo, 2\n\t"
    "s_cbranch_scc0 .Lgo0_%=\n\t"
    "s_sub_u32 s60, s60, 1\n\t"
    "s_cbranch_scc0 .Lpl0_%=\n\t"
    "s_mov_b64 vcc, 0\n\t"
    "s_branch .Lgo0_%=\n\t"
    ".Lpoll1_%=:\n\t"
    ".Lpl1_%=:\n\t"
    "s_mov_b64 exec, 0xff\n\t"
    "buffer_load_dword v248, v246, s[56:59], 0 offen sc1\n\t"
    "s_mov_b64 exec, -1\n\t"
    "s_waitcnt vmcnt(0)\n\t"
    "v_cmp_ne_u32 vcc, v248, v230\n\t"
    "s_and_b32 s64, vcc_lo, 12\n\t"
    "s_cbranch_scc0 .Lgo1_%=\n\t"
    "s_sub_u32 s60, s60, 1\n\t"
    "s_cbranch_scc0 .Lpl1_%=\n\t"
    "s_mov_b64 vcc, 0\n\t"
    "s_branch .Lgo1_%=\n\t"
    ".Lpoll2_%=:\n\t"
    ".Lpl2_%=:\n\t"
    "s_mov_b64 exec, 0xff\n\t"
    "buffer_load_dword v248, v246, s[56:59], 0 offen sc1\n\t"
    "s_mov_b64 exec, -1\n\t"
    "s_waitcnt vmcnt(0)\n\t"
    "v_cmp_ne_u32 vcc, v248, v230\n\t"
    "s_and_b32 s64, vcc_lo, 48\n\t"
    "s_cbranch_scc0 .Lgo2_%=\n\t"
    "s_sub_u32 s60, s60, 1\n\t"
    "s_cbranch_scc0 .Lpl2_%=\n\t"
    "s_mov_b64 vcc, 0\n\t"
    "s_branch .Lgo2_%=\n\t"
    ".Lpoll3_%=:\n\t"
    ".Lpl3_%=:\n\t"
    "s_mov_b64 exec, 0xff\n\t"
    "buffer_load_dword v248, v246, s[56:59], 0 offen sc1\n\t"
    "s_mov_b64 exec, -1\n\t"
    "s_waitcnt vmcnt(0)\n\t"
    "v_cmp_ne_u32 vcc, v248, v230\n\t"
    "s_and_b32 s64, vcc_lo, 192\n\t"
    "s_cbranch_scc0 .Lgo3_%=\n\t"
    "s_sub_u32 s60, s60, 1\n\t"
    "s_cbranch_scc0 .Lpl3_%=\n\t"
    "s_mov_b64 vcc, 0\n\t"
    "s_branch .Lgo3_%=\n\t"
    ".Lreecho%=:\n\t"
    "buffer_store_dword v120, v247, s[56:59], 0 offen sc1\n\t"
    "s_waitcnt vmcnt(0)\n\t"
    "s_branch .Lechoed%=\n\t"
    ".Lrestart%=:\n\t"
    "s_cmp_lg_u32 s67, 0\n\t"
    "s_cbranch_scc1 .Lnorst%=\n\t"
    "s_mov_b32 s67, 1\n\t"
    "s_waitcnt vmcnt(0) lgkmcnt(0)\n\t"
    "s_barrier\n\t"
    "s_branch .Lpass%=\n\t"
    ".Lreq2%=:\n\t"
    "buffer_load_dword v254, v246, s[56:59], 0 offen sc1\n\t"
    "s_branch .Lreq1%=\n\t"
    ".Lfix%=:\n\t"
    "s_waitcnt lgkmcnt(0)\n\t"
    "s_nop 7\n\t"
    "v_mfma_f32_16x16x32_bf16 v[144:147], a[64:67], a[0:3], 0\n\t"
    "v_mfma_f32_16x16x32_bf16 v[144:147], a[68:71], a[4:7], v[144:147]\n\t"
    "v_mfma_f32_16x16x32_bf16 v[144:147], a[72:75], a[8:11], v[144:147]\n\t"
    "v_mfma_f32_16x16x32_bf16 v[144:147], a[76:79], a[12:15], v[144:147]\n\t"
    "v_mfma_f32_16x16x32_bf16 v[148:151], a[80:83], a[0:3], 0\n\t"
    "v_mfma_f32_16x16x32_bf16 v[148:151], a[84:87], a[4:7], v[148:151]\n\t"
    "v_mfma_f32_16x16x32_bf16 v[148:151], a[88:91], a[8:11], v[148:151]\n\t"
    "v_mfma_f32_16x16x32_bf16 v[148:151], a[92:95], a[12:15], v[148:151]\n\t"
    "s_nop 7\n\t"
    "v_max3_f32 v249, v144, v145, v146\n\t"
    "v_max3_f32 v249, v249, v147, v148\n\t"
    "v_max3_f32 v249, v249, v149, v150\n\t"
    "v_max_f32 v249, v249, v151\n\t"
    "v_mov_b32 v250, v249\n\t"
    "s_nop 1\n\t"
    "v_permlane32_swap_b32 v249, v250\n\t"
    "s_nop 1\n\t"
    "v_max_f32 v249, v249, v250\n\t"
    "v_mov_b32 v250, v249\n\t"
    "s_nop 1\n\t"
    "v_permlane16_swap_b32 v249, v250\n\t"
    "s_nop 1\n\t"
    "v_max_f32 v249, v249, v250\n\t"
    "v_max_f32 v208, v208, v249\n\t"
    "v_mfma_f32_16x16x32_bf16 v[144:147], a[64:67], a[16:19], 0\n\t"
    "v_mfma_f32_16x16x32_bf16 v[144:147], a[68:71], a[20:23], v[144:147]\n\t"
    "v_mfma_f32_16x16x32_bf16 v[144:147], a[72:75], a[24:27], v[144:147]\n\t"
    "v_mfma_f32_16x16x32_bf16 v[144:147], a[76:79], a[28:31], v[144:147]\n\t"
    "v_mfma_f32_16x16x32_bf16 v[148:151], a[80:83], a[16:19], 0\n\t"
    "v_mfma_f32_16x16x32_bf16 v[148:151], a[84:87], a[20:23], v[148:151]\n\t"
    "v_mfma_f32_16x16x32_bf16 v[148:151], a[88:91], a[24:27], v[148:151]\n\t"
    "v_mfma_f32_16x16x32_bf16 v[148:151], a[92:95], a[28:31], v[148:151]\n\t"
    "s_nop 7\n\t"
    "v_max3_f32 v249, v144, v145, v146\n\t"
    "v_max3_f32 v249, v249, v147, v148\n\t"
    "v_max3_f32 v249, v249, v149, v150\n\t"
    "v_max_f32 v249, v249, v151\n\t"
    "v_mov_b32 v250, v249\n\t"
    "s_nop 1\n\t"
    "v_permlane32_swap_b32 v249, v250\n\t"
    "s_nop 1\n\t"
    "v_max_f32 v249, v249, v250\n\t"
    "v_mov_b32 v250, v249\n\t"
    "s_nop 1\n\t"
    "v_permlane16_swap_b32 v249, v250\n\t"
    "s_nop 1\n\t"
    "v_max_f32 v249, v249, v250\n\t"
    "v_max_f32 v212, v212, v249\n\t"
    "v_mfma_f32_16x16x32_bf16 v[144:147], a[64:67], a[32:35], 0\n\t"
    "v_mfma_f32_16x16x32_bf16 v[144:147], a[68:71], a[36:39], v[144:147]\n\t"
    "v_mfma_f32_16x16x32_bf16 v[144:147], a[72:75], a[40:43], v[144:147]\n\t"
    "v_mfma_f32_16x16x32_bf16 v[144:147], a[76:79], a[44:47], v[144:147]\n\t"
    "v_mfma_f32_16x16x32_bf16 v[148:151], a[80:83], a[32:35], 0\n\t"
    "v_mfma_f32_16x16x32_bf16 v[148:151], a[84:87], a[36:39], v[148:151]\n\t"
    "v_mfma_f32_16x16x32_bf16 v[148:151], a[88:91], a[40:43], v[148:151]\n\t"
    "v_mfma_f32_16x16x32_bf16 v[148:151], a[92:95], a[44:47], v[148:151]\n\t"
    "s_nop 7\n\t"
    "v_max3_f32 v249, v144, v145, v146\n\t"
    "v_max3_f32 v249, v249, v147, v148\n\t"
    "v_max3_f32 v249, v249, v149, v150\n\t"
    "v_max_f32 v249, v249, v151\n\t"
    "v_mov_b32 v250, v249\n\t"
    "s_nop 1\n\t"
    "v_permlane32_swap_b32 v249, v250\n\t"
    "s_nop 1\n\t"
    "v_max_f32 v249, v249, v250\n\t"
    "v_mov_b32 v250, v249\n\t"
    "s_nop 1\n\t"
    "v_permlane16_swap_b32 v249, v250\n\t"
    "s_nop 1\n\t"
    "v_max_f32 v249, v249, v250\n\t"
    "v_max_f32 v216, v216, v249\n\t"
    "v_mfma_f32_16x16x32_bf16 v[144:147], a[64:67], a[48:51], 0\n\t"
    "v_mfma_f32_16x16x32_bf16 v[144:147], a[68:71], a[52:55], v[144:147]\n\t"
    "v_mfma_f32_16x16x32_bf16 v[144:147], a[72:75], a[56:59], v[144:147]\n\t"
    "v_mfma_f32_16x16x32_bf16 v[144:147], a[76:79], a[60:63], v[144:147]\n\t"
    "v_mfma_f32_16x16x32_bf16 v[148:151], a[80:83], a[48:51], 0\n\t"
    "v_mfma_f32_16x16x32_bf16 v[148:151], a[84:87], a[52:55], v[148:151]\n\t"
    "v_mfma_f32_16x16x32_bf16 v[148:151], a[88:91], a[56:59], v[148:151]\n\t"
    "v_mfma_f32_16x16x32_bf16 v[148:151], a[92:95], a[60:63], v[148:151]\n\t"
    "s_nop 7\n\t"
    "v_max3_f32 v249, v144, v145, v146\n\t"
    "v_max3_f32 v249, v249, v147, v148\n\t"
    "v_max3_f32 v249, v249, v149, v150\n\t"
    "v_max_f32 v249, v249, v151\n\t"
    "v_mov_b32 v250, v249\n\t"
    "s_nop 1\n\t"
    "v_permlane32_swap_b32 v249, v250\n\t"
    "s_nop 1\n\t"
    "v_max_f32 v249, v249, v250\n\t"
    "v_mov_b32 v250, v249\n\t"
    "s_nop 1\n\t"
    "v_permlane16_swap_b32 v249, v250\n\t"
    "s_nop 1\n\t"
    "v_max_f32 v249, v249, v250\n\t"
    "v_max_f32 v220, v220, v249\n\t"
    "s_branch .Lnofix%=\n\t"
    ".Lsloop%=:\n\t"
    "v_max3_f32 v248, v200, v201, v202\n\t"
    "v_max3_f32 v248, v248, v203, v204\n\t"
    "v_max3_f32 v248, v248, v205, v206\n\t"
    "v_max_f32 v248, v248, v207\n\t"
    "v_cmp_lt_f32 vcc, s61, v248\n\t"
    "s_cbranch_vccnz .Lsite0_%=\n\t"
    ".Lback0_%=:\n\t"
    "v_mul_f32 v200, s53, v200\n\t"
    "v_mul_f32 v201, s53, v201\n\t"
    "v_mul_f32 v202, s53, v202\n\t"
    "v_mul_f32 v203, s53, v203\n\t"
    "v_mul_f32 v204, s53, v204\n\t"
    "v_mul_f32 v205, s53, v205\n\t"
    "v_mul_f32 v206, s53, v206\n\t"
    "v_mul_f32 v207, s53, v207\n\t"
    "v_mfma_f32_16x16x32_bf16 v[192:195], a[64:67], a[0:3], v[208:211]\n\t"
    "v_exp_f32 v200, v200\n\t"
    "v_mfma_f32_16x16x32_bf16 v[192:195], a[68:71], a[4:7], v[192:195]\n\t"
    "v_exp_f32 v201, v201\n\t"
    "v_mfma_f32_16x16x32_bf16 v[192:195], a[72:75], a[8:11], v[192:195]\n\t"
    "v_cvt_pk_bf16_f32 v188, v200, v201\n\t"
    "v_mfma_f32_16x16x32_bf16 v[192:195], a[76:79], a[12:15], v[192:195]\n\t"
    "ds_read_b64_tr_b16 a[144:145], v240 offset:49152\n\t"
    "ds_read_b64_tr_b16 a[146:147], v240 offset:53248\n\t"
    "v_mfma_f32_16x16x32_bf16 v[196:199], a[80:83], a[0:3], v[208:211]\n\t"
    "v_exp_f32 v202, v202\n\t"
    "v_mfma_f32_16x16x32_bf16 v[196:199], a[84:87], a[4:7], v[196:199]\n\t"
    "v_exp_f32 v203, v203\n\t"
    "v_mfma_f32_16x16x32_bf16 v[196:199], a[88:91], a[8:11], v[196:199]\n\t"
    "v_cvt_pk_bf16_f32 v189, v202, v203\n\t"
    "v_mfma_f32_16x16x32_bf16 v[196:199], a[92:95], a[12:15], v[196:199]\n\t"
    "s_add_u32 m0, s73, 0x14000\n\t"
    "v_mfma_f32_16x16x32_bf16 a[192:195], a[208:211], v[160:163], a[192:195]\n\t"
    "v_exp_f32 v204, v204\n\t"
    "v_mfma_f32_16x16x32_bf16 v[0:3], a[128:131], v[160:163], v[0:3]\n\t"
    "v_exp_f32 v205, v205\n\t"
    "v_mfma_f32_16x16x32_bf16 v[0:3], a[160:163], v[176:179], v[0:3]\n\t"
    "v_cvt_pk_bf16_f32 v190, v204, v205\n\t"
    "v_mfma_f32_16x16x32_bf16 v[32:35], a[128:131], v[164:167], v[32:35]\n\t"
    "ds_read_b64_tr_b16 a[176:177], v240 offset:57344\n\t"
    "ds_read_b64_tr_b16 a[178:179], v240 offset:61440\n\t"
    "v_mfma_f32_16x16x32_bf16 v[32:35], a[160:163], v[180:183], v[32:35]\n\t"
    "v_exp_f32 v206, v206\n\t"
    "v_mfma_f32_16x16x32_bf16 v[64:67], a[128:131], v[168:171], v[64:67]\n\t"
    "v_exp_f32 v207, v207\n\t"
    "v_mfma_f32_16x16x32_bf16 v[64:67], a[160:163], v[184:187], v[64:67]\n\t"
    "v_cvt_pk_bf16_f32 v191, v206, v207\n\t"
    "v_mfma_f32_16x16x32_bf16 v[96:99], a[128:131], v[172:175], v[96:99]\n\t"
    "buffer_load_dwordx4 v228, s[32:35], s60 offen nt lds\n\t"
    "v_mfma_f32_16x16x32_bf16 v[96:99], a[160:163], v[188:191], v[96:99]\n\t"
    "ds_read_b128 a[96:99], v232 offset:8192\n\t"
    "ds_read_b128 a[100:103], v233 offset:8192\n\t"
    "v_max3_f32 v248, v192, v193, v194\n\t"
    "v_max3_f32 v248, v248, v195, v196\n\t"
    "v_max3_f32 v248, v248, v197, v198\n\t"
    "v_max_f32 v248, v248, v199\n\t"
    "v_cmp_lt_f32 vcc, s61, v248\n\t"
    "s_cbranch_vccnz .Lsite1_%=\n\t"
    ".Lback1_%=:\n\t"
    "v_mul_f32 v192, s53, v192\n\t"
    "v_mul_f32 v193, s53, v193\n\t"
    "v_mul_f32 v194, s53, v194\n\t"
    "v_mul_f32 v195, s53, v195\n\t"
    "v_mul_f32 v196, s53, v196\n\t"
    "v_mul_f32 v197, s53, v197\n\t"
    "v_mul_f32 v198, s53, v198\n\t"
    "v_mul_f32 v199, s53, v199\n\t"
    "v_mfma_f32_16x16x32_bf16 v[200:203], a[64:67], a[16:19], v[212:215]\n\t"
    "v_exp_f32 v192, v192\n\t"
    "v_mfma_f32_16x16x32_bf16 v[200:203], a[68:71], a[20:23], v[200:203]\n\t"
    "v_exp_f32 v193, v193\n\t"
    "v_mfma_f32_16x16x32_bf16 v[200:203], a[72:75], a[24:27], v[200:203]\n\t"
    "v_cvt_pk_bf16_f32 v128, v192, v193\n\t"
    "v_mfma_f32_16x16x32_bf16 v[200:203], a[76:79], a[28:31], v[200:203]\n\t"
    "ds_read_b64_tr_b16 a[148:149], v241 offset:49152\n\t"
    "ds_read_b64_tr_b16 a[150:151], v241 offset:53248\n\t"
    "v_mfma_f32_16x16x32_bf16 v[204:207], a[80:83], a[16:19], v[212:215]\n\t"
    "v_exp_f32 v194, v194\n\t"
    "v_mfma_f32_16x16x32_bf16 v[204:207], a[84:87], a[20:23], v[204:207]\n\t"
    "v_exp_f32 v195, v195\n\t"
    "v_mfma_f32_16x16x32_bf16 v[204:207], a[88:91], a[24:27], v[204:207]\n\t"
    "v_cvt_pk_bf16_f32 v129, v194, v195\n\t"
    "v_mfma_f32_16x16x32_bf16 v[204:207], a[92:95], a[28:31], v[204:207]\n\t"
    "buffer_load_dwordx4 v229, s[32:35], s60 offen offset:1024 nt lds\n\t"
    "v_mfma_f32_16x16x32_bf16 a[196:199], a[208:211], v[164:167], a[196:199]\n\t"
    "v_exp_f32 v196, v196\n\t"
    "v_mfma_f32_16x16x32_bf16 v[4:7], a[132:135], v[160:163], v[4:7]\n\t"
    "v_exp_f32 v197, v197\n\t"
    "v_mfma_f32_16x16x32_bf16 v[4:7], a[164:167], v[176:179], v[4:7]\n\t"
    "v_cvt_pk_bf16_f32 v130, v196, v197\n\t"
    "v_mfma_f32_16x16x32_bf16 v[36:39], a[132:135], v[164:167], v[36:39]\n\t"
    "ds_read_b64_tr_b16 a[180:181], v241 offset:57344\n\t"
    "ds_read_b64_tr_b16 a[182:183], v241 offset:61440\n\t"
    "v_mfma_f32_16x16x32_bf16 v[36:39], a[164:167], v[180:183], v[36:39]\n\t"
    "v_exp_f32 v198, v198\n\t"
    "v_mfma_f32_16x16x32_bf16 v[68:71], a[132:135], v[168:171], v[68:71]\n\t"
    "v_exp_f32 v199, v199\n\t"
    "v_mfma_f32_16x16x32_bf16 v[68:71], a[164:167], v[184:187], v[68:71]\n\t"
    "v_cvt_pk_bf16_f32 v131, v198, v199\n\t"
    "v_mfma_f32_16x16x32_bf16 v[100:103], a[132:135], v[172:175], v[100:103]\n\t"
    "v_mfma_f32_16x16x32_bf16 v[100:103], a[164:167], v[188:191], v[100:103]\n\t"
    "ds_read_b128 a[104:107], v234 offset:8192\n\t"
    "ds_read_b128 a[108:111], v235 offset:8192\n\t"
    "v_max3_f32 v248, v200, v201, v202\n\t"
    "v_max3_f32 v248, v248, v203, v204\n\t"
    "v_max3_f32 v248, v248, v205, v206\n\t"
    "v_max_f32 v248, v248, v207\n\t"
    "v_cmp_lt_f32 vcc, s61, v248\n\t"
    "s_cbranch_vccnz .Lsite2_%=\n\t"
    ".Lback2_%=:\n\t"
    "v_mul_f32 v200, s53, v200\n\t"
    "v_mul_f32 v201, s53, v201\n\t"
    "v_mul_f32 v202, s53, v202\n\t"
    "v_mul_f32 v203, s53, v203\n\t"
    "v_mul_f32 v204, s53, v204\n\t"
    "v_mul_f32 v205, s53, v205\n\t"
    "v_mul_f32 v206, s53, v206\n\t"
    "v_mul_f32 v207, s53, v207\n\t"
    "v_mfma_f32_16x16x32_bf16 v[192:195], a[64:67], a[32:35], v[216:219]\n\t"
    "v_exp_f32 v200, v200\n\t"
    "v_mfma_f32_16x16x32_bf16 v[192:195], a[68:71], a[36:39], v[192:195]\n\t"
    "v_exp_f32 v201, v201\n\t"
    "v_mfma_f32_16x16x32_bf16 v[192:195], a[72:75], a[40:43], v[192:195]\n\t"
    "v_cvt_pk_bf16_f32 v132, v200, v201\n\t"
    "v_mfma_f32_16x16x32_bf16 v[192:195], a[76:79], a[44:47], v[192:195]\n\t"
    "ds_read_b64_tr_b16 a[152:153], v242 offset:49152\n\t"
    "ds_read_b64_tr_b16 a[154:155], v242 offset:53248\n\t"
    "v_mfma_f32_16x16x32_bf16 v[196:199], a[80:83], a[32:35], v[216:219]\n\t"
    "v_exp_f32 v202, v202\n\t"
    "v_mfma_f32_16x16x32_bf16 v[196:199], a[84:87], a[36:39], v[196:199]\n\t"
    "v_exp_f32 v203, v203\n\t"
    "v_mfma_f32_16x16x32_bf16 v[196:199], a[88:91], a[40:43], v[196:199]\n\t"
    "v_cvt_pk_bf16_f32 v133, v202, v203\n\t"
    "v_mfma_f32_16x16x32_bf16 v[196:199], a[92:95], a[44:47], v[196:199]\n\t"
    "buffer_load_dwordx4 v228, s[32:35], s60 offen offset:2048 nt lds\n\t"
    "v_mfma_f32_16x16x32_bf16 a[200:203], a[208:211], v[168:171], a[200:203]\n\t"
    "v_exp_f32 v204, v204\n\t"
    "v_mfma_f32_16x16x32_bf16 v[8:11], a[136:139], v[160:163], v[8:11]\n\t"
    "v_exp_f32 v205, v205\n\t"
    "v_mfma_f32_16x16x32_bf16 v[8:11], a[168:171], v[176:179], v[8:11]\n\t"
    "v_cvt_pk_bf16_f32 v134, v204, v205\n\t"
    "v_mfma_f32_16x16x32_bf16 v[40:43], a[136:139], v[164:167], v[40:43]\n\t"
    "ds_read_b64_tr_b16 a[184:185], v242 offset:57344\n\t"
    "ds_read_b64_tr_b16 a[186:187], v242 offset:61440\n\t"
    "v_mfma_f32_16x16x32_bf16 v[40:43], a[168:171], v[180:183], v[40:43]\n\t"
    "v_exp_f32 v206, v206\n\t"
    "v_mfma_f32_16x16x32_bf16 v[72:75], a[136:139], v[168:171], v[72:75]\n\t"
    "v_exp_f32 v207, v207\n\t"
    "v_mfma_f32_16x16x32_bf16 v[72:75], a[168:171], v[184:187], v[72:75]\n\t"
    "v_cvt_pk_bf16_f32 v135, v206, v207\n\t"
    "v_mfma_f32_16x16x32_bf16 v[104:107], a[136:139], v[172:175], v[104:107]\n\t"
    "buffer_load_dwordx4 v229, s[32:35], s60 offen offset:3072 nt lds\n\t"
    "v_mfma_f32_16x16x32_bf16 v[104:107], a[168:171], v[188:191], v[104:107]\n\t"
    "ds_read_b128 a[112:115], v232 offset:12288\n\t"
    "ds_read_b128 a[116:119], v233 offset:12288\n\t"
    "v_max3_f32 v248, v192, v193, v194\n\t"
    "v_max3_f32 v248, v248, v195, v196\n\t"
    "v_max3_f32 v248, v248, v197, v198\n\t"
    "v_max_f32 v248, v248, v199\n\t"
    "v_cmp_lt_f32 vcc, s61, v248\n\t"
    "s_cbranch_vccnz .Lsite3_%=\n\t"
    ".Lback3_%=:\n\t"
    "v_mul_f32 v192, s53, v192\n\t"
    "v_mul_f32 v193, s53, v193\n\t"
    "v_mul_f32 v194, s53, v194\n\t"
    "v_mul_f32 v195, s53, v195\n\t"
    "v_mul_f32 v196, s53, v196\n\t"
    "v_mul_f32 v197, s53, v197\n\t"
    "v_mul_f32 v198, s53, v198\n\t"
    "v_mul_f32 v199, s53, v199\n\t"
    "v_mfma_f32_16x16x32_bf16 v[200:203], a[64:67], a[48:51], v[220:223]\n\t"
    "v_exp_f32 v192, v192\n\t"
    "v_mfma_f32_16x16x32_bf16 v[200:203], a[68:71], a[52:55], v[200:203]\n\t"
    "v_exp_f32 v193, v193\n\t"
    "v_mfma_f32_16x16x32_bf16 v[200:203], a[72:75], a[56:59], v[200:203]\n\t"
    "v_cvt_pk_bf16_f32 v136, v192, v193\n\t"
    "v_mfma_f32_16x16x32_bf16 v[200:203], a[76:79], a[60:63], v[200:203]\n\t"
    "ds_read_b64_tr_b16 a[156:157], v243 offset:49152\n\t"
    "ds_read_b64_tr_b16 a[158:159], v243 offset:53248\n\t"
    "v_mfma_f32_16x16x32_bf16 v[204:207], a[80:83], a[48:51], v[220:223]\n\t"
    "v_exp_f32 v194, v194\n\t"
    "v_mfma_f32_16x16x32_bf16 v[204:207], a[84:87], a[52:55], v[204:207]\n\t"
    "v_exp_f32 v195, v195\n\t"
    "v_mfma_f32_16x16x32_bf16 v[204:207], a[88:91], a[56:59], v[204:207]\n\t"
    "v_cvt_pk_bf16_f32 v137, v194, v195\n\t"
    "v_mfma_f32_16x16x32_bf16 v[204:207], a[92:95], a[60:63], v[204:207]\n\t"
    "ds_read_b64_tr_b16 a[188:189], v243 offset:57344\n\t"
    "ds_read_b64_tr_b16 a[190:191], v243 offset:61440\n\t"
    "v_mfma_f32_16x16x32_bf16 a[204:207], a[208:211], v[172:175], a[204:207]\n\t"
    "v_exp_f32 v196, v196\n\t"
    "v_mfma_f32_16x16x32_bf16 v[12:15], a[140:143], v[160:163], v[12:15]\n\t"
    "v_exp_f32 v197, v197\n\t"
    "v_mfma_f32_16x16x32_bf16 v[12:15], a[172:175], v[176:179], v[12:15]\n\t"
    "v_cvt_pk_bf16_f32 v138, v196, v197\n\t"
    "v_mfma_f32_16x16x32_bf16 v[44:47], a[140:143], v[164:167], v[44:47]\n\t"
    "ds_read_b128 a[120:123], v234 offset:12288\n\t"
    "ds_read_b128 a[124:127], v235 offset:12288\n\t"
    "v_mfma_f32_16x16x32_bf16 v[44:47], a[172:175], v[180:183], v[44:47]\n\t"
    "v_exp_f32 v198, v198\n\t"
    "v_mfma_f32_16x16x32_bf16 v[76:79], a[140:143], v[168:171], v[76:79]\n\t"
    "v_exp_f32 v199, v199\n\t"
    "v_mfma_f32_16x16x32_bf16 v[76:79], a[172:175], v[184:187], v[76:79]\n\t"
    "v_cvt_pk_bf16_f32 v139, v198, v199\n\t"
    "v_mfma_f32_16x16x32_bf16 v[108:111], a[140:143], v[172:175], v[108:111]\n\t"
    "s_add_u32 s60, s60, 0x4000\n\t"
    "v_mfma_f32_16x16x32_bf16 v[108:111], a[172:175], v[188:191], v[108:111]\n\t"
    "s_waitcnt vmcnt(8) lgkmcnt(0)\n\t"
    "s_barrier\n\t"
    "v_max3_f32 v248, v200, v201, v202\n\t"
    "v_max3_f32 v248, v248, v203, v204\n\t"
    "v_max3_f32 v248, v248, v205, v206\n\t"
    "v_max_f32 v248, v248, v207\n\t"
    "v_cmp_lt_f32 vcc, s61, v248\n\t"
    "s_cbranch_vccnz .Lsite4_%=\n\t"
    ".Lback4_%=:\n\t"
    "v_mul_f32 v200, s53, v200\n\t"
    "v_mul_f32 v201, s53, v201\n\t"
    "v_mul_f32 v202, s53, v202\n\t"
    "v_mul_f32 v203, s53, v203\n\t"
    "v_mul_f32 v204, s53, v204\n\t"
    "v_mul_f32 v205, s53, v205\n\t"
    "v_mul_f32 v206, s53, v206\n\t"
    "v_mul_f32 v207, s53, v207\n\t"
    "v_mfma_f32_16x16x32_bf16 v[192:195], a[96:99], a[0:3], v[208:211]\n\t"
    "v_exp_f32 v200, v200\n\t"
    "v_mfma_f32_16x16x32_bf16 v[192:195], a[100:103], a[4:7], v[192:195]\n\t"
    "v_exp_f32 v201, v201\n\t"
    "v_mfma_f32_16x16x32_bf16 v[192:195], a[104:107], a[8:11], v[192:195]\n\t"
    "v_cvt_pk_bf16_f32 v140, v200, v201\n\t"
    "v_mfma_f32_16x16x32_bf16 v[192:195], a[108:111], a[12:15], v[192:195]\n\t"
    "ds_read_b64_tr_b16 a[128:129], v236 offset:0\n\t"
    "ds_read_b64_tr_b16 a[130:131], v236 offset:4096\n\t"
    "v_mfma_f32_16x16x32_bf16 v[196:199], a[112:115], a[0:3], v[208:211]\n\t"
    "v_exp_f32 v202, v202\n\t"
    "v_mfma_f32_16x16x32_bf16 v[196:199], a[116:119], a[4:7], v[196:199]\n\t"
    "v_exp_f32 v203, v203\n\t"
    "v_mfma_f32_16x16x32_bf16 v[196:199], a[120:123], a[8:11], v[196:199]\n\t"
    "v_cvt_pk_bf16_f32 v141, v202, v203\n\t"
    "v_mfma_f32_16x16x32_bf16 v[196:199], a[124:127], a[12:15], v[196:199]\n\t"
    "s_add_u32 m0, s73, 0xc000\n\t"
    "v_mfma_f32_16x16x32_bf16 a[192:195], a[208:211], v[176:179], a[192:195]\n\t"
    "v_exp_f32 v204, v204\n\t"
    "v_mfma_f32_16x16x32_bf16 v[16:19], a[144:147], v[160:163], v[16:19]\n\t"
    "v_exp_f32 v205, v205\n\t"
    "v_mfma_f32_16x16x32_bf16 v[16:19], a[176:179], v[176:179], v[16:19]\n\t"
    "v_cvt_pk_bf16_f32 v142, v204, v205\n\t"
    "v_mfma_f32_16x16x32_bf16 v[48:51], a[144:147], v[164:167], v[48:51]\n\t"
    "ds_read_b64_tr_b16 a[160:161], v236 offset:8192\n\t"
    "ds_read_b64_tr_b16 a[162:163], v236 offset:12288\n\t"
    "v_mfma_f32_16x16x32_bf16 v[48:51], a[176:179], v[180:183], v[48:51]\n\t"
    "v_exp_f32 v206, v206\n\t"
    "v_mfma_f32_16x16x32_bf16 v[80:83], a[144:147], v[168:171], v[80:83]\n\t"
    "v_exp_f32 v207, v207\n\t"
    "v_mfma_f32_16x16x32_bf16 v[80:83], a[176:179], v[184:187], v[80:83]\n\t"
    "v_cvt_pk_bf16_f32 v143, v206, v207\n\t"
    "v_mfma_f32_16x16x32_bf16 v[112:115], a[144:147], v[172:175], v[112:115]\n\t"
    "buffer_load_dwordx4 v224, s[28:31], s62 offen nt lds\n\t"
    "v_mfma_f32_16x16x32_bf16 v[112:115], a[176:179], v[188:191], v[112:115]\n\t"
    "ds_read_b128 a[64:67], v232 offset:16384\n\t"
    "ds_read_b128 a[68:71], v233 offset:16384\n\t"
    "v_max3_f32 v248, v192, v193, v194\n\t"
    "v_max3_f32 v248, v248, v195, v196\n\t"
    "v_max3_f32 v248, v248, v197, v198\n\t"
    "v_max_f32 v248, v248, v199\n\t"
    "v_cmp_lt_f32 vcc, s61, v248\n\t"
    "s_cbranch_vccnz .Lsite5_%=\n\t"
    ".Lback5_%=:\n\t"
    "v_mul_f32 v192, s53, v192\n\t"
    "v_mul_f32 v193, s53, v193\n\t"
    "v_mul_f32 v194, s53, v194\n\t"
    "v_mul_f32 v195, s53, v195\n\t"
    "v_mul_f32 v196, s53, v196\n\t"
    "v_mul_f32 v197, s53, v197\n\t"
    "v_mul_f32 v198, s53, v198\n\t"
    "v_mul_f32 v199, s53, v199\n\t"
    "v_mfma_f32_16x16x32_bf16 v[200:203], a[96:99], a[16:19], v[212:215]\n\t"
    "v_exp_f32 v192, v192\n\t"
    "v_mfma_f32_16x16x32_bf16 v[200:203], a[100:103], a[20:23], v[200:203]\n\t"
    "v_exp_f32 v193, v193\n\t"
    "v_mfma_f32_16x16x32_bf16 v[200:203], a[104:107], a[24:27], v[200:203]\n\t"
    "v_cvt_pk_bf16_f32 v144, v192, v193\n\t"
    "v_mfma_f32_16x16x32_bf16 v[200:203], a[108:111], a[28:31], v[200:203]\n\t"
    "ds_read_b64_tr_b16 a[132:133], v237 offset:0\n\t"
    "ds_read_b64_tr_b16 a[134:135], v237 offset:4096\n\t"
    "v_mfma_f32_16x16x32_bf16 v[204:207], a[112:115], a[16:19], v[212:215]\n\t"
    "v_exp_f32 v194, v194\n\t"
    "v_mfma_f32_16x16x32_bf16 v[204:207], a[116:119], a[20:23], v[204:207]\n\t"
    "v_exp_f32 v195, v195\n\t"
    "v_mfma_f32_16x16x32_bf16 v[204:207], a[120:123], a[24:27], v[204:207]\n\t"
    "v_cvt_pk_bf16_f32 v145, v194, v195\n\t"
    "v_mfma_f32_16x16x32_bf16 v[204:207], a[124:127], a[28:31], v[204:207]\n\t"
    "buffer_load_dwordx4 v225, s[28:31], s62 offen offset:1024 nt lds\n\t"
    "v_mfma_f32_16x16x32_bf16 a[196:199], a[208:211], v[180:183], a[196:199]\n\t"
    "v_exp_f32 v196, v196\n\t"
    "v_mfma_f32_16x16x32_bf16 v[20:23], a[148:151], v[160:163], v[20:23]\n\t"
    "v_exp_f32 v197, v197\n\t"
    "v_mfma_f32_16x16x32_bf16 v[20:23], a[180:183], v[176:179], v[20:23]\n\t"
    "v_cvt_pk_bf16_f32 v146, v196, v197\n\t"
    "v_mfma_f32_16x16x32_bf16 v[52:55], a[148:151], v[164:167], v[52:55]\n\t"
    "ds_read_b64_tr_b16 a[164:165], v237 offset:8192\n\t"
    "ds_read_b64_tr_b16 a[166:167], v237 offset:12288\n\t"
    "v_mfma_f32_16x16x32_bf16 v[52:55], a[180:183], v[180:183], v[52:55]\n\t"
    "v_exp_f32 v198, v198\n\t"
    "v_mfma_f32_16x16x32_bf16 v[84:87], a[148:151], v[168:171], v[84:87]\n\t"
    "v_exp_f32 v199, v199\n\t"
    "v_mfma_f32_16x16x32_bf16 v[84:87], a[180:183], v[184:187], v[84:87]\n\t"
    "v_cvt_pk_bf16_f32 v147, v198, v199\n\t"
    "v_mfma_f32_16x16x32_bf16 v[116:119], a[148:151], v[172:175], v[116:119]\n\t"
    "v_mfma_f32_16x16x32_bf16 v[116:119], a[180:183], v[188:191], v[116:119]\n\t"
    "ds_read_b128 a[72:75], v234 offset:16384\n\t"
    "ds_read_b128 a[76:79], v235 offset:16384\n\t"
    "v_max3_f32 v248, v200, v201, v202\n\t"
    "v_max3_f32 v248, v248, v203, v204\n\t"
    "v_max3_f32 v248, v248, v205, v206\n\t"
    "v_max_f32 v248, v248, v207\n\t"
    "v_cmp_lt_f32 vcc, s61, v248\n\t"
    "s_cbranch_vccnz .Lsite6_%=\n\t"
    ".Lback6_%=:\n\t"
    "v_mul_f32 v200, s53, v200\n\t"
    "v_mul_f32 v201, s53, v201\n\t"
    "v_mul_f32 v202, s53, v202\n\t"
    "v_mul_f32 v203, s53, v203\n\t"
    "v_mul_f32 v204, s53, v204\n\t"
    "v_mul_f32 v205, s53, v205\n\t"
    "v_mul_f32 v206, s53, v206\n\t"
    "v_mul_f32 v207, s53, v207\n\t"
    "v_mfma_f32_16x16x32_bf16 v[192:195], a[96:99], a[32:35], v[216:219]\n\t"
    "v_exp_f32 v200, v200\n\t"
    "v_mfma_f32_16x16x32_bf16 v[192:195], a[100:103], a[36:39], v[192:195]\n\t"
    "v_exp_f32 v201, v201\n\t"
    "v_mfma_f32_16x16x32_bf16 v[192:195], a[104:107], a[40:43], v[192:195]\n\t"
    "v_cvt_pk_bf16_f32 v148, v200, v201\n\t"
    "v_mfma_f32_16x16x32_bf16 v[192:195], a[108:111], a[44:47], v[192:195]\n\t"
    "ds_read_b64_tr_b16 a[136:137], v238 offset:0\n\t"
    "ds_read_b64_tr_b16 a[138:139], v238 offset:4096\n\t"
    "v_mfma_f32_16x16x32_bf16 v[196:199], a[112:115], a[32:35], v[216:219]\n\t"
    "v_exp_f32 v202, v202\n\t"
    "v_mfma_f32_16x16x32_bf16 v[196:199], a[116:119], a[36:39], v[196:199]\n\t"
    "v_exp_f32 v203, v203\n\t"
    "v_mfma_f32_16x16x32_bf16 v[196:199], a[120:123], a[40:43], v[196:199]\n\t"
    "v_cvt_pk_bf16_f32 v149, v202, v203\n\t"
    "v_mfma_f32_16x16x32_bf16 v[196:199], a[124:127], a[44:47], v[196:199]\n\t"
    "buffer_load_dwordx4 v226, s[28:31], s62 offen offset:2048 nt lds\n\t"
    "v_mfma_f32_16x16x32_bf16 a[200:203], a[208:211], v[184:187], a[200:203]\n\t"
    "v_exp_f32 v204, v204\n\t"
    "v_mfma_f32_16x16x32_bf16 v[24:27], a[152:155], v[160:163], v[24:27]\n\t"
    "v_exp_f32 v205, v205\n\t"
    "v_mfma_f32_16x16x32_bf16 v[24:27], a[184:187], v[176:179], v[24:27]\n\t"
    "v_cvt_pk_bf16_f32 v150, v204, v205\n\t"
    "v_mfma_f32_16x16x32_bf16 v[56:59], a[152:155], v[164:167], v[56:59]\n\t"
    "ds_read_b64_tr_b16 a[168:169], v238 offset:8192\n\t"
    "ds_read_b64_tr_b16 a[170:171], v238 offset:12288\n\t"
    "v_mfma_f32_16x16x32_bf16 v[56:59], a[184:187], v[180:183], v[56:59]\n\t"
    "v_exp_f32 v206, v206\n\t"
    "v_mfma_f32_16x16x32_bf16 v[88:91], a[152:155], v[168:171], v[88:91]\n\t"
    "v_exp_f32 v207, v207\n\t"
    "v_mfma_f32_16x16x32_bf16 v[88:91], a[184:187], v[184:187], v[88:91]\n\t"
    "v_cvt_pk_bf16_f32 v151, v206, v207\n\t"
    "v_mfma_f32_16x16x32_bf16 v[120:123], a[152:155], v[172:175], v[120:123]\n\t"
    "buffer_load_dwordx4 v227, s[28:31], s62 offen offset:3072 nt lds\n\t"
    "v_mfma_f32_16x16x32_bf16 v[120:123], a[184:187], v[188:191], v[120:123]\n\t"
    "ds_read_b128 a[80:83], v232 offset:20480\n\t"
    "ds_read_b128 a[84:87], v233 offset:20480\n\t"
    "v_max3_f32 v248, v192, v193, v194\n\t"
    "v_max3_f32 v248, v248, v195, v196\n\t"
    "v_max3_f32 v248, v248, v197, v198\n\t"
    "v_max_f32 v248, v248, v199\n\t"
    "v_cmp_lt_f32 vcc, s61, v248\n\t"
    "s_cbranch_vccnz .Lsite7_%=\n\t"
    ".Lback7_%=:\n\t"
    "v_mul_f32 v192, s53, v192\n\t"
    "v_mul_f32 v193, s53, v193\n\t"
    "v_mul_f32 v194, s53, v194\n\t"
    "v_mul_f32 v195, s53, v195\n\t"
    "v_mul_f32 v196, s53, v196\n\t"
    "v_mul_f32 v197, s53, v197\n\t"
    "v_mul_f32 v198, s53, v198\n\t"
    "v_mul_f32 v199, s53, v199\n\t"
    "v_mfma_f32_16x16x32_bf16 v[200:203], a[96:99], a[48:51], v[220:223]\n\t"
    "v_exp_f32 v192, v192\n\t"
    "v_mfma_f32_16x16x32_bf16 v[200:203], a[100:103], a[52:55], v[200:203]\n\t"
    "v_exp_f32 v193, v193\n\t"
    "v_mfma_f32_16x16x32_bf16 v[200:203], a[104:107], a[56:59], v[200:203]\n\t"
    "v_cvt_pk_bf16_f32 v152, v192, v193\n\t"
    "v_mfma_f32_16x16x32_bf16 v[200:203], a[108:111], a[60:63], v[200:203]\n\t"
    "ds_read_b64_tr_b16 a[140:141], v239 offset:0\n\t"
    "ds_read_b64_tr_b16 a[142:143], v239 offset:4096\n\t"
    "v_mfma_f32_16x16x32_bf16 v[204:207], a[112:115], a[48:51], v[220:223]\n\t"
    "v_exp_f32 v194, v194\n\t"
    "v_mfma_f32_16x16x32_bf16 v[204:207], a[116:119], a[52:55], v[204:207]\n\t"
    "v_exp_f32 v195, v195\n\t"
    "v_mfma_f32_16x16x32_bf16 v[204:207], a[120:123], a[56:59], v[204:207]\n\t"
    "v_cvt_pk_bf16_f32 v153, v194, v195\n\t"
    "v_mfma_f32_16x16x32_bf16 v[204:207], a[124:127], a[60:63], v[204:207]\n\t"
    "ds_read_b64_tr_b16 a[172:173], v239 offset:8192\n\t"
    "ds_read_b64_tr_b16 a[174:175], v239 offset:12288\n\t"
    "v_mfma_f32_16x16x32_bf16 a[204:207], a[208:211], v[188:191], a[204:207]\n\t"
    "v_exp_f32 v196, v196\n\t"
    "v_mfma_f32_16x16x32_bf16 v[28:31], a[156:159], v[160:163], v[28:31]\n\t"
    "v_exp_f32 v197, v197\n\t"
    "v_mfma_f32_16x16x32_bf16 v[28:31], a[188:191], v[176:179], v[28:31]\n\t"
    "v_cvt_pk_bf16_f32 v154, v196, v197\n\t"
    "v_mfma_f32_16x16x32_bf16 v[60:63], a[156:159], v[164:167], v[60:63]\n\t"
    "ds_read_b128 a[88:91], v234 offset:20480\n\t"
    "ds_read_b128 a[92:95], v235 offset:20480\n\t"
    "v_mfma_f32_16x16x32_bf16 v[60:63], a[188:191], v[180:183], v[60:63]\n\t"
    "v_exp_f32 v198, v198\n\t"
    "v_mfma_f32_16x16x32_bf16 v[92:95], a[156:159], v[168:171], v[92:95]\n\t"
    "v_exp_f32 v199, v199\n\t"
    "v_mfma_f32_16x16x32_bf16 v[92:95], a[188:191], v[184:187], v[92:95]\n\t"
    "v_cvt_pk_bf16_f32 v155, v198, v199\n\t"
    "v_mfma_f32_16x16x32_bf16 v[124:127], a[156:159], v[172:175], v[124:127]\n\t"
    "s_add_u32 s62, s62, 0x4000\n\t"
    "v_mfma_f32_16x16x32_bf16 v[124:127], a[188:191], v[188:191], v[124:127]\n\t"
    "s_waitcnt lgkmcnt(0)\n\t"
    "v_max3_f32 v248, v200, v201, v202\n\t"
    "v_max3_f32 v248, v248, v203, v204\n\t"
    "v_max3_f32 v248, v248, v205, v206\n\t"
    "v_max_f32 v248, v248, v207\n\t"
    "v_cmp_lt_f32 vcc, s61, v248\n\t"
    "s_cbranch_vccnz .Lsite8_%=\n\t"
    ".Lback8_%=:\n\t"
    "v_mul_f32 v200, s53, v200\n\t"
    "v_mul_f32 v201, s53, v201\n\t"
    "v_mul_f32 v202, s53, v202\n\t"
    "v_mul_f32 v203, s53, v203\n\t"
    "v_mul_f32 v204, s53, v204\n\t"
    "v_mul_f32 v205, s53, v205\n\t"
    "v_mul_f32 v206, s53, v206\n\t"
    "v_mul_f32 v207, s53, v207\n\t"
    "v_mfma_f32_16x16x32_bf16 v[192:195], a[64:67], a[0:3], v[208:211]\n\t"
    "v_exp_f32 v200, v200\n\t"
    "v_mfma_f32_16x16x32_bf16 v[192:195], a[68:71], a[4:7], v[192:195]\n\t"
    "v_exp_f32 v201, v201\n\t"
    "v_mfma_f32_16x16x32_bf16 v[192:195], a[72:75], a[8:11], v[192:195]\n\t"
    "v_cvt_pk_bf16_f32 v156, v200, v201\n\t"
    "v_mfma_f32_16x16x32_bf16 v[192:195], a[76:79], a[12:15], v[192:195]\n\t"
    "ds_read_b64_tr_b16 a[144:145], v240 offset:0\n\t"
    "ds_read_b64_tr_b16 a[146:147], v240 offset:4096\n\t"
    "v_mfma_f32_16x16x32_bf16 v[196:199], a[80:83], a[0:3], v[208:211]\n\t"
    "v_exp_f32 v202, v202\n\t"
    "v_mfma_f32_16x16x32_bf16 v[196:199], a[84:87], a[4:7], v[196:199]\n\t"
    "v_exp_f32 v203, v203\n\t"
    "v_mfma_f32_16x16x32_bf16 v[196:199], a[88:91], a[8:11], v[196:199]\n\t"
    "v_cvt_pk_bf16_f32 v157, v202, v203\n\t"
    "v_mfma_f32_16x16x32_bf16 v[196:199], a[92:95], a[12:15], v[196:199]\n\t"
    "s_add_u32 m0, s73, 0x18000\n\t"
    "v_mfma_f32_16x16x32_bf16 a[192:195], a[208:211], v[128:131], a[192:195]\n\t"
    "v_exp_f32 v204, v204\n\t"
    "v_mfma_f32_16x16x32_bf16 v[0:3], a[128:131], v[128:131], v[0:3]\n\t"
    "v_exp_f32 v205, v205\n\t"
    "v_mfma_f32_16x16x32_bf16 v[0:3], a[160:163], v[144:147], v[0:3]\n\t"
    "v_cvt_pk_bf16_f32 v158, v204, v205\n\t"
    "v_mfma_f32_16x16x32_bf16 v[32:35], a[128:131], v[132:135], v[32:35]\n\t"
    "ds_read_b64_tr_b16 a[176:177], v240 offset:8192\n\t"
    "ds_read_b64_tr_b16 a[178:179], v240 offset:12288\n\t"
    "v_mfma_f32_16x16x32_bf16 v[32:35], a[160:163], v[148:151], v[32:35]\n\t"
    "v_exp_f32 v206, v206\n\t"
    "v_mfma_f32_16x16x32_bf16 v[64:67], a[128:131], v[136:139], v[64:67]\n\t"
    "v_exp_f32 v207, v207\n\t"
    "v_mfma_f32_16x16x32_bf16 v[64:67], a[160:163], v[152:155], v[64:67]\n\t"
    "v_cvt_pk_bf16_f32 v159, v206, v207\n\t"
    "v_mfma_f32_16x16x32_bf16 v[96:99], a[128:131], v[140:143], v[96:99]\n\t"
    "buffer_load_dwordx4 v228, s[32:35], s60 offen nt lds\n\t"
    "v_mfma_f32_16x16x32_bf16 v[96:99], a[160:163], v[156:159], v[96:99]\n\t"
    "ds_read_b128 a[96:99], v232 offset:24576\n\t"
    "ds_read_b128 a[100:103], v233 offset:24576\n\t"
    "v_max3_f32 v248, v192, v193, v194\n\t"
    "v_max3_f32 v248, v248, v195, v196\n\t"
    "v_max3_f32 v248, v248, v197, v198\n\t"
    "v_max_f32 v248, v248, v199\n\t"
    "v_cmp_lt_f32 vcc, s61, v248\n\t"
    "s_cbranch_vccnz .Lsite9_%=\n\t"
    ".Lback9_%=:\n\t"
    "v_mul_f32 v192, s53, v192\n\t"
    "v_mul_f32 v193, s53, v193\n\t"
    "v_mul_f32 v194, s53, v194\n\t"
    "v_mul_f32 v195, s53, v195\n\t"
    "v_mul_f32 v196, s53, v196\n\t"
    "v_mul_f32 v197, s53, v197\n\t"
    "v_mul_f32 v198, s53, v198\n\t"
    "v_mul_f32 v199, s53, v199\n\t"
    "v_mfma_f32_16x16x32_bf16 v[200:203], a[64:67], a[16:19], v[212:215]\n\t"
    "v_exp_f32 v192, v192\n\t"
    "v_mfma_f32_16x16x32_bf16 v[200:203], a[68:71], a[20:23], v[200:203]\n\t"
    "v_exp_f32 v193, v193\n\t"
    "v_mfma_f32_16x16x32_bf16 v[200:203], a[72:75], a[24:27], v[200:203]\n\t"
    "v_cvt_pk_bf16_f32 v160, v192, v193\n\t"
    "v_mfma_f32_16x16x32_bf16 v[200:203], a[76:79], a[28:31], v[200:203]\n\t"
    "ds_read_b64_tr_b16 a[148:149], v241 offset:0\n\t"
    "ds_read_b64_tr_b16 a[150:151], v241 offset:4096\n\t"
    "v_mfma_f32_16x16x32_bf16 v[204:207], a[80:83], a[16:19], v[212:215]\n\t"
    "v_exp_f32 v194, v194\n\t"
    "v_mfma_f32_16x16x32_bf16 v[204:207], a[84:87], a[20:23], v[204:207]\n\t"
    "v_exp_f32 v195, v195\n\t"
    "v_mfma_f32_16x16x32_bf16 v[204:207], a[88:91], a[24:27], v[204:207]\n\t"
    "v_cvt_pk_bf16_f32 v161, v194, v195\n\t"
    "v_mfma_f32_16x16x32_bf16 v[204:207], a[92:95], a[28:31], v[204:207]\n\t"
    "buffer_load_dwordx4 v229, s[32:35], s60 offen offset:1024 nt lds\n\t"
    "v_mfma_f32_16x16x32_bf16 a[196:199], a[208:211], v[132:135], a[196:199]\n\t"
    "v_exp_f32 v196, v196\n\t"
    "v_mfma_f32_16x16x32_bf16 v[4:7], a[132:135], v[128:131], v[4:7]\n\t"
    "v_exp_f32 v197, v197\n\t"
    "v_mfma_f32_16x16x32_bf16 v[4:7], a[164:167], v[144:147], v[4:7]\n\t"
    "v_cvt_pk_bf16_f32 v162, v196, v197\n\t"
    "v_mfma_f32_16x16x32_bf16 v[36:39], a[132:135], v[132:135], v[36:39]\n\t"
    "ds_read_b64_tr_b16 a[180:181], v241 offset:8192\n\t"
    "ds_read_b64_tr_b16 a[182:183], v241 offset:12288\n\t"
    "v_mfma_f32_16x16x32_bf16 v[36:39], a[164:167], v[148:151], v[36:39]\n\t"
    "v_exp_f32 v198, v198\n\t"
    "v_mfma_f32_16x16x32_bf16 v[68:71], a[132:135], v[136:139], v[68:71]\n\t"
    "v_exp_f32 v199, v199\n\t"
    "v_mfma_f32_16x16x32_bf16 v[68:71], a[164:167], v[152:155], v[68:71]\n\t"
    "v_cvt_pk_bf16_f32 v163, v198, v199\n\t"
    "v_mfma_f32_16x16x32_bf16 v[100:103], a[132:135], v[140:143], v[100:103]\n\t"
    "v_mfma_f32_16x16x32_bf16 v[100:103], a[164:167], v[156:159], v[100:103]\n\t"
    "ds_read_b128 a[104:107], v234 offset:24576\n\t"
    "ds_read_b128 a[108:111], v235 offset:24576\n\t"
    "v_max3_f32 v248, v200, v201, v202\n\t"
    "v_max3_f32 v248, v248, v203, v204\n\t"
    "v_max3_f32 v248, v248, v205, v206\n\t"
    "v_max_f32 v248, v248, v207\n\t"
    "v_cmp_lt_f32 vcc, s61, v248\n\t"
    "s_cbranch_vccnz .Lsite10_%=\n\t"
    ".Lback10_%=:\n\t"
    "v_mul_f32 v200, s53, v200\n\t"
    "v_mul_f32 v201, s53, v201\n\t"
    "v_mul_f32 v202, s53, v202\n\t"
    "v_mul_f32 v203, s53, v203\n\t"
    "v_mul_f32 v204, s53, v204\n\t"
    "v_mul_f32 v205, s53, v205\n\t"
    "v_mul_f32 v206, s53, v206\n\t"
    "v_mul_f32 v207, s53, v207\n\t"
    "v_mfma_f32_16x16x32_bf16 v[192:195], a[64:67], a[32:35], v[216:219]\n\t"
    "v_exp_f32 v200, v200\n\t"
    "v_mfma_f32_16x16x32_bf16 v[192:195], a[68:71], a[36:39], v[192:195]\n\t"
    "v_exp_f32 v201, v201\n\t"
    "v_mfma_f32_16x16x32_bf16 v[192:195], a[72:75], a[40:43], v[192:195]\n\t"
    "v_cvt_pk_bf16_f32 v164, v200, v201\n\t"
    "v_mfma_f32_16x16x32_bf16 v[192:195], a[76:79], a[44:47], v[192:195]\n\t"
    "ds_read_b64_tr_b16 a[152:153], v242 offset:0\n\t"
    "ds_read_b64_tr_b16 a[154:155], v242 offset:4096\n\t"
    "v_mfma_f32_16x16x32_bf16 v[196:199], a[80:83], a[32:35], v[216:219]\n\t"
    "v_exp_f32 v202, v202\n\t"
    "v_mfma_f32_16x16x32_bf16 v[196:199], a[84:87], a[36:39], v[196:199]\n\t"
    "v_exp_f32 v203, v203\n\t"
    "v_mfma_f32_16x16x32_bf16 v[196:199], a[88:91], a[40:43], v[196:199]\n\t"
    "v_cvt_pk_bf16_f32 v165, v202, v203\n\t"
    "v_mfma_f32_16x16x32_bf16 v[196:199], a[92:95], a[44:47], v[196:199]\n\t"
    "buffer_load_dwordx4 v228, s[32:35], s60 offen offset:2048 nt lds\n\t"
    "v_mfma_f32_16x16x32_bf16 a[200:203], a[208:211], v[136:139], a[200:203]\n\t"
    "v_exp_f32 v204, v204\n\t"
    "v_mfma_f32_16x16x32_bf16 v[8:11], a[136:139], v[128:131], v[8:11]\n\t"
    "v_exp_f32 v205, v205\n\t"
    "v_mfma_f32_16x16x32_bf16 v[8:11], a[168:171], v[144:147], v[8:11]\n\t"
    "v_cvt_pk_bf16_f32 v166, v204, v205\n\t"
    "v_mfma_f32_16x16x32_bf16 v[40:43], a[136:139], v[132:135], v[40:43]\n\t"
    "ds_read_b64_tr_b16 a[184:185], v242 offset:8192\n\t"
    "ds_read_b64_tr_b16 a[186:187], v242 offset:12288\n\t"
    "v_mfma_f32_16x16x32_bf16 v[40:43], a[168:171], v[148:151], v[40:43]\n\t"
    "v_exp_f32 v206, v206\n\t"
    "v_mfma_f32_16x16x32_bf16 v[72:75], a[136:139], v[136:139], v[72:75]\n\t"
    "v_exp_f32 v207, v207\n\t"
    "v_mfma_f32_16x16x32_bf16 v[72:75], a[168:171], v[152:155], v[72:75]\n\t"
    "v_cvt_pk_bf16_f32 v167, v206, v207\n\t"
    "v_mfma_f32_16x16x32_bf16 v[104:107], a[136:139], v[140:143], v[104:107]\n\t"
    "buffer_load_dwordx4 v229, s[32:35], s60 offen offset:3072 nt lds\n\t"
    "v_mfma_f32_16x16x32_bf16 v[104:107], a[168:171], v[156:159], v[104:107]\n\t"
    "ds_read_b128 a[112:115], v232 offset:28672\n\t"
    "ds_read_b128 a[116:119], v233 offset:28672\n\t"
    "v_max3_f32 v248, v192, v193, v194\n\t"
    "v_max3_f32 v248, v248, v195, v196\n\t"
    "v_max3_f32 v248, v248, v197, v198\n\t"
    "v_max_f32 v248, v248, v199\n\t"
    "v_cmp_lt_f32 vcc, s61, v248\n\t"
    "s_cbranch_vccnz .Lsite11_%=\n\t"
    ".Lback11_%=:\n\t"
    "v_mul_f32 v192, s53, v192\n\t"
    "v_mul_f32 v193, s53, v193\n\t"
    "v_mul_f32 v194, s53, v194\n\t"
    "v_mul_f32 v195, s53, v195\n\t"
    "v_mul_f32 v196, s53, v196\n\t"
    "v_mul_f32 v197, s53, v197\n\t"
    "v_mul_f32 v198, s53, v198\n\t"
    "v_mul_f32 v199, s53, v199\n\t"
    "v_mfma_f32_16x16x32_bf16 v[200:203], a[64:67], a[48:51], v[220:223]\n\t"
    "v_exp_f32 v192, v192\n\t"
    "v_mfma_f32_16x16x32_bf16 v[200:203], a[68:71], a[52:55], v[200:203]\n\t"
    "v_exp_f32 v193, v193\n\t"
    "v_mfma_f32_16x16x32_bf16 v[200:203], a[72:75], a[56:59], v[200:203]\n\t"
    "v_cvt_pk_bf16_f32 v168, v192, v193\n\t"
    "v_mfma_f32_16x16x32_bf16 v[200:203], a[76:79], a[60:63], v[200:203]\n\t"
    "ds_read_b64_tr_b16 a[156:157], v243 offset:0\n\t"
    "ds_read_b64_tr_b16 a[158:159], v243 offset:4096\n\t"
    "v_mfma_f32_16x16x32_bf16 v[204:207], a[80:83], a[48:51], v[220:223]\n\t"
    "v_exp_f32 v194, v194\n\t"
    "v_mfma_f32_16x16x32_bf16 v[204:207], a[84:87], a[52:55], v[204:207]\n\t"
    "v_exp_f32 v195, v195\n\t"
    "v_mfma_f32_16x16x32_bf16 v[204:207], a[88:91], a[56:59], v[204:207]\n\t"
    "v_cvt_pk_bf16_f32 v169, v194, v195\n\t"
    "v_mfma_f32_16x16x32_bf16 v[204:207], a[92:95], a[60:63], v[204:207]\n\t"
    "ds_read_b64_tr_b16 a[188:189], v243 offset:8192\n\t"
    "ds_read_b64_tr_b16 a[190:191], v243 offset:12288\n\t"
    "v_mfma_f32_16x16x32_bf16 a[204:207], a[208:211], v[140:143], a[204:207]\n\t"
    "v_exp_f32 v196, v196\n\t"
    "v_mfma_f32_16x16x32_bf16 v[12:15], a[140:143], v[128:131], v[12:15]\n\t"
    "v_exp_f32 v197, v197\n\t"
    "v_mfma_f32_16x16x32_bf16 v[12:15], a[172:175], v[144:147], v[12:15]\n\t"
    "v_cvt_pk_bf16_f32 v170, v196, v197\n\t"
    "v_mfma_f32_16x16x32_bf16 v[44:47], a[140:143], v[132:135], v[44:47]\n\t"
    "ds_read_b128 a[120:123], v234 offset:28672\n\t"
    "ds_read_b128 a[124:127], v235 offset:28672\n\t"
    "v_mfma_f32_16x16x32_bf16 v[44:47], a[172:175], v[148:151], v[44:47]\n\t"
    "v_exp_f32 v198, v198\n\t"
    "v_mfma_f32_16x16x32_bf16 v[76:79], a[140:143], v[136:139], v[76:79]\n\t"
    "v_exp_f32 v199, v199\n\t"
    "v_mfma_f32_16x16x32_bf16 v[76:79], a[172:175], v[152:155], v[76:79]\n\t"
    "v_cvt_pk_bf16_f32 v171, v198, v199\n\t"
    "v_mfma_f32_16x16x32_bf16 v[108:111], a[140:143], v[140:143], v[108:111]\n\t"
    "s_add_u32 s60, s60, 0x4000\n\t"
    "v_mfma_f32_16x16x32_bf16 v[108:111], a[172:175], v[156:159], v[108:111]\n\t"
    "s_waitcnt vmcnt(8) lgkmcnt(0)\n\t"
    "s_barrier\n\t"
    "v_max3_f32 v248, v200, v201, v202\n\t"
    "v_max3_f32 v248, v248, v203, v204\n\t"
    "v_max3_f32 v248, v248, v205, v206\n\t"
    "v_max_f32 v248, v248, v207\n\t"
    "v_cmp_lt_f32 vcc, s61, v248\n\t"
    "s_cbranch_vccnz .Lsite12_%=\n\t"
    ".Lback12_%=:\n\t"
    "v_mul_f32 v200, s53, v200\n\t"
    "v_mul_f32 v201, s53, v201\n\t"
    "v_mul_f32 v202, s53, v202\n\t"
    "v_mul_f32 v203, s53, v203\n\t"
    "v_mul_f32 v204, s53, v204\n\t"
    "v_mul_f32 v205, s53, v205\n\t"
    "v_mul_f32 v206, s53, v206\n\t"
    "v_mul_f32 v207, s53, v207\n\t"
    "v_mfma_f32_16x16x32_bf16 v[192:195], a[96:99], a[0:3], v[208:211]\n\t"
    "v_exp_f32 v200, v200\n\t"
    "v_mfma_f32_16x16x32_bf16 v[192:195], a[100:103], a[4:7], v[192:195]\n\t"
    "v_exp_f32 v201, v201\n\t"
    "v_mfma_f32_16x16x32_bf16 v[192:195], a[104:107], a[8:11], v[192:195]\n\t"
    "v_cvt_pk_bf16_f32 v172, v200, v201\n\t"
    "v_mfma_f32_16x16x32_bf16 v[192:195], a[108:111], a[12:15], v[192:195]\n\t"
    "ds_read_b64_tr_b16 a[128:129], v236 offset:16384\n\t"
    "ds_read_b64_tr_b16 a[130:131], v236 offset:20480\n\t"
    "v_mfma_f32_16x16x32_bf16 v[196:199], a[112:115], a[0:3], v[208:211]\n\t"
    "v_exp_f32 v202, v202\n\t"
    "v_mfma_f32_16x16x32_bf16 v[196:199], a[116:119], a[4:7], v[196:199]\n\t"
    "v_exp_f32 v203, v203\n\t"
    "v_mfma_f32_16x16x32_bf16 v[196:199], a[120:123], a[8:11], v[196:199]\n\t"
    "v_cvt_pk_bf16_f32 v173, v202, v203\n\t"
    "v_mfma_f32_16x16x32_bf16 v[196:199], a[124:127], a[12:15], v[196:199]\n\t"
    "s_add_u32 m0, s73, 0x0\n\t"
    "v_mfma_f32_16x16x32_bf16 a[192:195], a[208:211], v[144:147], a[192:195]\n\t"
    "v_exp_f32 v204, v204\n\t"
    "v_mfma_f32_16x16x32_bf16 v[16:19], a[144:147], v[128:131], v[16:19]\n\t"
    "v_exp_f32 v205, v205\n\t"
    "v_mfma_f32_16x16x32_bf16 v[16:19], a[176:179], v[144:147], v[16:19]\n\t"
    "v_cvt_pk_bf16_f32 v174, v204, v205\n\t"
    "v_mfma_f32_16x16x32_bf16 v[48:51], a[144:147], v[132:135], v[48:51]\n\t"
    "ds_read_b64_tr_b16 a[160:161], v236 offset:24576\n\t"
    "ds_read_b64_tr_b16 a[162:163], v236 offset:28672\n\t"
    "v_mfma_f32_16x16x32_bf16 v[48:51], a[176:179], v[148:151], v[48:51]\n\t"
    "v_exp_f32 v206, v206\n\t"
    "v_mfma_f32_16x16x32_bf16 v[80:83], a[144:147], v[136:139], v[80:83]\n\t"
    "v_exp_f32 v207, v207\n\t"
    "v_mfma_f32_16x16x32_bf16 v[80:83], a[176:179], v[152:155], v[80:83]\n\t"
    "v_cvt_pk_bf16_f32 v175, v206, v207\n\t"
    "v_mfma_f32_16x16x32_bf16 v[112:115], a[144:147], v[140:143], v[112:115]\n\t"
    "buffer_load_dwordx4 v224, s[28:31], s62 offen nt lds\n\t"
    "v_mfma_f32_16x16x32_bf16 v[112:115], a[176:179], v[156:159], v[112:115]\n\t"
    "ds_read_b128 a[64:67], v232 offset:32768\n\t"
    "ds_read_b128 a[68:71], v233 offset:32768\n\t"
    "v_max3_f32 v248, v192, v193, v194\n\t"
    "v_max3_f32 v248, v248, v195, v196\n\t"
    "v_max3_f32 v248, v248, v197, v198\n\t"
    "v_max_f32 v248, v248, v199\n\t"
    "v_cmp_lt_f32 vcc, s61, v248\n\t"
    "s_cbranch_vccnz .Lsite13_%=\n\t"
    ".Lback13_%=:\n\t"
    "v_mul_f32 v192, s53, v192\n\t"
    "v_mul_f32 v193, s53, v193\n\t"
    "v_mul_f32 v194, s53, v194\n\t"
    "v_mul_f32 v195, s53, v195\n\t"
    "v_mul_f32 v196, s53, v196\n\t"
    "v_mul_f32 v197, s53, v197\n\t"
    "v_mul_f32 v198, s53, v198\n\t"
    "v_mul_f32 v199, s53, v199\n\t"
    "v_mfma_f32_16x16x32_bf16 v[200:203], a[96:99], a[16:19], v[212:215]\n\t"
    "v_exp_f32 v192, v192\n\t"
    "v_mfma_f32_16x16x32_bf16 v[200:203], a[100:103], a[20:23], v[200:203]\n\t"
    "v_exp_f32 v193, v193\n\t"
    "v_mfma_f32_16x16x32_bf16 v[200:203], a[104:107], a[24:27], v[200:203]\n\t"
    "v_cvt_pk_bf16_f32 v176, v192, v193\n\t"
    "v_mfma_f32_16x16x32_bf16 v[200:203], a[108:111], a[28:31], v[200:203]\n\t"
    "ds_read_b64_tr_b16 a[132:133], v237 offset:16384\n\t"
    "ds_read_b64_tr_b16 a[134:135], v237 offset:20480\n\t"
    "v_mfma_f32_16x16x32_bf16 v[204:207], a[112:115], a[16:19], v[212:215]\n\t"
    "v_exp_f32 v194, v194\n\t"
    "v_mfma_f32_16x16x32_bf16 v[204:207], a[116:119], a[20:23], v[204:207]\n\t"
    "v_exp_f32 v195, v195\n\t"
    "v_mfma_f32_16x16x32_bf16 v[204:207], a[120:123], a[24:27], v[204:207]\n\t"
    "v_cvt_pk_bf16_f32 v177, v194, v195\n\t"
    "v_mfma_f32_16x16x32_bf16 v[204:207], a[124:127], a[28:31], v[204:207]\n\t"
    "buffer_load_dwordx4 v225, s[28:31], s62 offen offset:1024 nt lds\n\t"
    "v_mfma_f32_16x16x32_bf16 a[196:199], a[208:211], v[148:151], a[196:199]\n\t"
    "v_exp_f32 v196, v196\n\t"
    "v_mfma_f32_16x16x32_bf16 v[20:23], a[148:151], v[128:131], v[20:23]\n\t"
    "v_exp_f32 v197, v197\n\t"
    "v_mfma_f32_16x16x32_bf16 v[20:23], a[180:183], v[144:147], v[20:23]\n\t"
    "v_cvt_pk_bf16_f32 v178, v196, v197\n\t"
    "v_mfma_f32_16x16x32_bf16 v[52:55], a[148:151], v[132:135], v[52:55]\n\t"
    "ds_read_b64_tr_b16 a[164:165], v237 offset:24576\n\t"
    "ds_read_b64_tr_b16 a[166:167], v237 offset:28672\n\t"
    "v_mfma_f32_16x16x32_bf16 v[52:55], a[180:183], v[148:151], v[52:55]\n\t"
    "v_exp_f32 v198, v198\n\t"
    "v_mfma_f32_16x16x32_bf16 v[84:87], a[148:151], v[136:139], v[84:87]\n\t"
    "v_exp_f32 v199, v199\n\t"
    "v_mfma_f32_16x16x32_bf16 v[84:87], a[180:183], v[152:155], v[84:87]\n\t"
    "v_cvt_pk_bf16_f32 v179, v198, v199\n\t"
    "v_mfma_f32_16x16x32_bf16 v[116:119], a[148:151], v[140:143], v[116:119]\n\t"
    "v_mfma_f32_16x16x32_bf16 v[116:119], a[180:183], v[156:159], v[116:119]\n\t"
    "ds_read_b128 a[72:75], v234 offset:32768\n\t"
    "ds_read_b128 a[76:79], v235 offset:32768\n\t"
    "v_max3_f32 v248, v200, v201, v202\n\t"
    "v_max3_f32 v248, v248, v203, v204\n\t"
    "v_max3_f32 v248, v248, v205, v206\n\t"
    "v_max_f32 v248, v248, v207\n\t"
    "v_cmp_lt_f32 vcc, s61, v248\n\t"
    "s_cbranch_vccnz .Lsite14_%=\n\t"
    ".Lback14_%=:\n\t"
    "v_mul_f32 v200, s53, v200\n\t"
    "v_mul_f32 v201, s53, v201\n\t"
    "v_mul_f32 v202, s53, v202\n\t"
    "v_mul_f32 v203, s53, v203\n\t"
    "v_mul_f32 v204, s53, v204\n\t"
    "v_mul_f32 v205, s53, v205\n\t"
    "v_mul_f32 v206, s53, v206\n\t"
    "v_mul_f32 v207, s53, v207\n\t"
    "v_mfma_f32_16x16x32_bf16 v[192:195], a[96:99], a[32:35], v[216:219]\n\t"
    "v_exp_f32 v200, v200\n\t"
    "v_mfma_f32_16x16x32_bf16 v[192:195], a[100:103], a[36:39], v[192:195]\n\t"
    "v_exp_f32 v201, v201\n\t"
    "v_mfma_f32_16x16x32_bf16 v[192:195], a[104:107], a[40:43], v[192:195]\n\t"
    "v_cvt_pk_bf16_f32 v180, v200, v201\n\t"
    "v_mfma_f32_16x16x32_bf16 v[192:195], a[108:111], a[44:47], v[192:195]\n\t"
    "ds_read_b64_tr_b16 a[136:137], v238 offset:16384\n\t"
    "ds_read_b64_tr_b16 a[138:139], v238 offset:20480\n\t"
    "v_mfma_f32_16x16x32_bf16 v[196:199], a[112:115], a[32:35], v[216:219]\n\t"
    "v_exp_f32 v202, v202\n\t"
    "v_mfma_f32_16x16x32_bf16 v[196:199], a[116:119], a[36:39], v[196:199]\n\t"
    "v_exp_f32 v203, v203\n\t"
    "v_mfma_f32_16x16x32_bf16 v[196:199], a[120:123], a[40:43], v[196:199]\n\t"
    "v_cvt_pk_bf16_f32 v181, v202, v203\n\t"
    "v_mfma_f32_16x16x32_bf16 v[196:199], a[124:127], a[44:47], v[196:199]\n\t"
    "buffer_load_dwordx4 v226, s[28:31], s62 offen offset:2048 nt lds\n\t"
    "v_mfma_f32_16x16x32_bf16 a[200:203], a[208:211], v[152:155], a[200:203]\n\t"
    "v_exp_f32 v204, v204\n\t"
    "v_mfma_f32_16x16x32_bf16 v[24:27], a[152:155], v[128:131], v[24:27]\n\t"
    "v_exp_f32 v205, v205\n\t"
    "v_mfma_f32_16x16x32_bf16 v[24:27], a[184:187], v[144:147], v[24:27]\n\t"
    "v_cvt_pk_bf16_f32 v182, v204, v205\n\t"
    "v_mfma_f32_16x16x32_bf16 v[56:59], a[152:155], v[132:135], v[56:59]\n\t"
    "ds_read_b64_tr_b16 a[168:169], v238 offset:24576\n\t"
    "ds_read_b64_tr_b16 a[170:171], v238 offset:28672\n\t"
    "v_mfma_f32_16x16x32_bf16 v[56:59], a[184:187], v[148:151], v[56:59]\n\t"
    "v_exp_f32 v206, v206\n\t"
    "v_mfma_f32_16x16x32_bf16 v[88:91], a[152:155], v[136:139], v[88:91]\n\t"
    "v_exp_f32 v207, v207\n\t"
    "v_mfma_f32_16x16x32_bf16 v[88:91], a[184:187], v[152:155], v[88:91]\n\t"
    "v_cvt_pk_bf16_f32 v183, v206, v207\n\t"
    "v_mfma_f32_16x16x32_bf16 v[120:123], a[152:155], v[140:143], v[120:123]\n\t"
    "buffer_load_dwordx4 v227, s[28:31], s62 offen offset:3072 nt lds\n\t"
    "v_mfma_f32_16x16x32_bf16 v[120:123], a[184:187], v[156:159], v[120:123]\n\t"
    "ds_read_b128 a[80:83], v232 offset:36864\n\t"
    "ds_read_b128 a[84:87], v233 offset:36864\n\t"
    "v_max3_f32 v248, v192, v193, v194\n\t"
    "v_max3_f32 v248, v248, v195, v196\n\t"
    "v_max3_f32 v248, v248, v197, v198\n\t"
    "v_max_f32 v248, v248, v199\n\t"
    "v_cmp_lt_f32 vcc, s61, v248\n\t"
    "s_cbranch_vccnz .Lsite15_%=\n\t"
    ".Lback15_%=:\n\t"
    "v_mul_f32 v192, s53, v192\n\t"
    "v_mul_f32 v193, s53, v193\n\t"
    "v_mul_f32 v194, s53, v194\n\t"
    "v_mul_f32 v195, s53, v195\n\t"
    "v_mul_f32 v196, s53, v196\n\t"
    "v_mul_f32 v197, s53, v197\n\t"
    "v_mul_f32 v198, s53, v198\n\t"
    "v_mul_f32 v199, s53, v199\n\t"
    "v_mfma_f32_16x16x32_bf16 v[200:203], a[96:99], a[48:51], v[220:223]\n\t"
    "v_exp_f32 v192, v192\n\t"
    "v_mfma_f32_16x16x32_bf16 v[200:203], a[100:103], a[52:55], v[200:203]\n\t"
    "v_exp_f32 v193, v193\n\t"
    "v_mfma_f32_16x16x32_bf16 v[200:203], a[104:107], a[56:59], v[200:203]\n\t"
    "v_cvt_pk_bf16_f32 v184, v192, v193\n\t"
    "v_mfma_f32_16x16x32_bf16 v[200:203], a[108:111], a[60:63], v[200:203]\n\t"
    "ds_read_b64_tr_b16 a[140:141], v239 offset:16384\n\t"
    "ds_read_b64_tr_b16 a[142:143], v239 offset:20480\n\t"
    "v_mfma_f32_16x16x32_bf16 v[204:207], a[112:115], a[48:51], v[220:223]\n\t"
    "v_exp_f32 v194, v194\n\t"
    "v_mfma_f32_16x16x32_bf16 v[204:207], a[116:119], a[52:55], v[204:207]\n\t"
    "v_exp_f32 v195, v195\n\t"
    "v_mfma_f32_16x16x32_bf16 v[204:207], a[120:123], a[56:59], v[204:207]\n\t"
    "v_cvt_pk_bf16_f32 v185, v194, v195\n\t"
    "v_mfma_f32_16x16x32_bf16 v[204:207], a[124:127], a[60:63], v[204:207]\n\t"
    "ds_read_b64_tr_b16 a[172:173], v239 offset:24576\n\t"
    "ds_read_b64_tr_b16 a[174:175], v239 offset:28672\n\t"
    "v_mfma_f32_16x16x32_bf16 a[204:207], a[208:211], v[156:159], a[204:207]\n\t"
    "v_exp_f32 v196, v196\n\t"
    "v_mfma_f32_16x16x32_bf16 v[28:31], a[156:159], v[128:131], v[28:31]\n\t"
    "v_exp_f32 v197, v197\n\t"
    "v_mfma_f32_16x16x32_bf16 v[28:31], a[188:191], v[144:147], v[28:31]\n\t"
    "v_cvt_pk_bf16_f32 v186, v196, v197\n\t"
    "v_mfma_f32_16x16x32_bf16 v[60:63], a[156:159], v[132:135], v[60:63]\n\t"
    "ds_read_b128 a[88:91], v234 offset:36864\n\t"
    "ds_read_b128 a[92:95], v235 offset:36864\n\t"
    "v_mfma_f32_16x16x32_bf16 v[60:63], a[188:191], v[148:151], v[60:63]\n\t"
    "v_exp_f32 v198, v198\n\t"
    "v_mfma_f32_16x16x32_bf16 v[92:95], a[156:159], v[136:139], v[92:95]\n\t"
    "v_exp_f32 v199, v199\n\t"
    "v_mfma_f32_16x16x32_bf16 v[92:95], a[188:191], v[152:155], v[92:95]\n\t"
    "v_cvt_pk_bf16_f32 v187, v198, v199\n\t"
    "v_mfma_f32_16x16x32_bf16 v[124:127], a[156:159], v[140:143], v[124:127]\n\t"
    "s_add_u32 s62, s62, 0x4000\n\t"
    "v_mfma_f32_16x16x32_bf16 v[124:127], a[188:191], v[156:159], v[124:127]\n\t"
    "s_waitcnt lgkmcnt(0)\n\t"
    "v_max3_f32 v248, v200, v201, v202\n\t"
    "v_max3_f32 v248, v248, v203, v204\n\t"
    "v_max3_f32 v248, v248, v205, v206\n\t"
    "v_max_f32 v248, v248, v207\n\t"
    "v_cmp_lt_f32 vcc, s61, v248\n\t"
    "s_cbranch_vccnz .Lsite16_%=\n\t"
    ".Lback16_%=:\n\t"
    "v_mul_f32 v200, s53, v200\n\t"
    "v_mul_f32 v201, s53, v201\n\t"
    "v_mul_f32 v202, s53, v202\n\t"
    "v_mul_f32 v203, s53, v203\n\t"
    "v_mul_f32 v204, s53, v204\n\t"
    "v_mul_f32 v205, s53, v205\n\t"
    "v_mul_f32 v206, s53, v206\n\t"
    "v_mul_f32 v207, s53, v207\n\t"
    "v_mfma_f32_16x16x32_bf16 v[192:195], a[64:67], a[0:3], v[208:211]\n\t"
    "v_exp_f32 v200, v200\n\t"
    "v_mfma_f32_16x16x32_bf16 v[192:195], a[68:71], a[4:7], v[192:195]\n\t"
    "v_exp_f32 v201, v201\n\t"
    "v_mfma_f32_16x16x32_bf16 v[192:195], a[72:75], a[8:11], v[192:195]\n\t"
    "v_cvt_pk_bf16_f32 v188, v200, v201\n\t"
    "v_mfma_f32_16x16x32_bf16 v[192:195], a[76:79], a[12:15], v[192:195]\n\t"
    "ds_read_b64_tr_b16 a[144:145], v240 offset:16384\n\t"
    "ds_read_b64_tr_b16 a[146:147], v240 offset:20480\n\t"
    "v_mfma_f32_16x16x32_bf16 v[196:199], a[80:83], a[0:3], v[208:211]\n\t"
    "v_exp_f32 v202, v202\n\t"
    "v_mfma_f32_16x16x32_bf16 v[196:199], a[84:87], a[4:7], v[196:199]\n\t"
    "v_exp_f32 v203, v203\n\t"
    "v_mfma_f32_16x16x32_bf16 v[196:199], a[88:91], a[8:11], v[196:199]\n\t"
    "v_cvt_pk_bf16_f32 v189, v202, v203\n\t"
    "v_mfma_f32_16x16x32_bf16 v[196:199], a[92:95], a[12:15], v[196:199]\n\t"
    "s_add_u32 m0, s73, 0x1c000\n\t"
    "v_mfma_f32_16x16x32_bf16 a[192:195], a[208:211], v[160:163], a[192:195]\n\t"
    "v_exp_f32 v204, v204\n\t"
    "v_mfma_f32_16x16x32_bf16 v[0:3], a[128:131], v[160:163], v[0:3]\n\t"
    "v_exp_f32 v205, v205\n\t"
    "v_mfma_f32_16x16x32_bf16 v[0:3], a[160:163], v[176:179], v[0:3]\n\t"
    "v_cvt_pk_bf16_f32 v190, v204, v205\n\t"
    "v_mfma_f32_16x16x32_bf16 v[32:35], a[128:131], v[164:167], v[32:35]\n\t"
    "ds_read_b64_tr_b16 a[176:177], v240 offset:24576\n\t"
    "ds_read_b64_tr_b16 a[178:179], v240 offset:28672\n\t"
    "v_mfma_f32_16x16x32_bf16 v[32:35], a[160:163], v[180:183], v[32:35]\n\t"
    "v_exp_f32 v206, v206\n\t"
    "v_mfma_f32_16x16x32_bf16 v[64:67], a[128:131], v[168:171], v[64:67]\n\t"
    "v_exp_f32 v207, v207\n\t"
    "v_mfma_f32_16x16x32_bf16 v[64:67], a[160:163], v[184:187], v[64:67]\n\t"
    "v_cvt_pk_bf16_f32 v191, v206, v207\n\t"
    "v_mfma_f32_16x16x32_bf16 v[96:99], a[128:131], v[172:175], v[96:99]\n\t"
    "buffer_load_dwordx4 v228, s[32:35], s60 offen nt lds\n\t"
    "v_mfma_f32_16x16x32_bf16 v[96:99], a[160:163], v[188:191], v[96:99]\n\t"
    "ds_read_b128 a[96:99], v232 offset:40960\n\t"
    "ds_read_b128 a[100:103], v233 offset:40960\n\t"
    "v_max3_f32 v248, v192, v193, v194\n\t"
    "v_max3_f32 v248, v248, v195, v196\n\t"
    "v_max3_f32 v248, v248, v197, v198\n\t"
    "v_max_f32 v248, v248, v199\n\t"
    "v_cmp_lt_f32 vcc, s61, v248\n\t"
    "s_cbranch_vccnz .Lsite17_%=\n\t"
    ".Lback17_%=:\n\t"
    "v_mul_f32 v192, s53, v192\n\t"
    "v_mul_f32 v193, s53, v193\n\t"
    "v_mul_f32 v194, s53, v194\n\t"
    "v_mul_f32 v195, s53, v195\n\t"
    "v_mul_f32 v196, s53, v196\n\t"
    "v_mul_f32 v197, s53, v197\n\t"
    "v_mul_f32 v198, s53, v198\n\t"
    "v_mul_f32 v199, s53, v199\n\t"
    "v_mfma_f32_16x16x32_bf16 v[200:203], a[64:67], a[16:19], v[212:215]\n\t"
    "v_exp_f32 v192, v192\n\t"
    "v_mfma_f32_16x16x32_bf16 v[200:203], a[68:71], a[20:23], v[200:203]\n\t"
    "v_exp_f32 v193, v193\n\t"
    "v_mfma_f32_16x16x32_bf16 v[200:203], a[72:75], a[24:27], v[200:203]\n\t"
    "v_cvt_pk_bf16_f32 v128, v192, v193\n\t"
    "v_mfma_f32_16x16x32_bf16 v[200:203], a[76:79], a[28:31], v[200:203]\n\t"
    "ds_read_b64_tr_b16 a[148:149], v241 offset:16384\n\t"
    "ds_read_b64_tr_b16 a[150:151], v241 offset:20480\n\t"
    "v_mfma_f32_16x16x32_bf16 v[204:207], a[80:83], a[16:19], v[212:215]\n\t"
    "v_exp_f32 v194, v194\n\t"
    "v_mfma_f32_16x16x32_bf16 v[204:207], a[84:87], a[20:23], v[204:207]\n\t"
    "v_exp_f32 v195, v195\n\t"
    "v_mfma_f32_16x16x32_bf16 v[204:207], a[88:91], a[24:27], v[204:207]\n\t"
    "v_cvt_pk_bf16_f32 v129, v194, v195\n\t"
    "v_mfma_f32_16x16x32_bf16 v[204:207], a[92:95], a[28:31], v[204:207]\n\t"
    "buffer_load_dwordx4 v229, s[32:35], s60 offen offset:1024 nt lds\n\t"
    "v_mfma_f32_16x16x32_bf16 a[196:199], a[208:211], v[164:167], a[196:199]\n\t"
    "v_exp_f32 v196, v196\n\t"
    "v_mfma_f32_16x16x32_bf16 v[4:7], a[132:135], v[160:163], v[4:7]\n\t"
    "v_exp_f32 v197, v197\n\t"
    "v_mfma_f32_16x16x32_bf16 v[4:7], a[164:167], v[176:179], v[4:7]\n\t"
    "v_cvt_pk_bf16_f32 v130, v196, v197\n\t"
    "v_mfma_f32_16x16x32_bf16 v[36:39], a[132:135], v[164:167], v[36:39]\n\t"
    "ds_read_b64_tr_b16 a[180:181], v241 offset:24576\n\t"
    "ds_read_b64_tr_b16 a[182:183], v241 offset:28672\n\t"
    "v_mfma_f32_16x16x32_bf16 v[36:39], a[164:167], v[180:183], v[36:39]\n\t"
    "v_exp_f32 v198, v198\n\t"
    "v_mfma_f32_16x16x32_bf16 v[68:71], a[132:135], v[168:171], v[68:71]\n\t"
    "v_exp_f32 v199, v199\n\t"
    "v_mfma_f32_16x16x32_bf16 v[68:71], a[164:167], v[184:187], v[68:71]\n\t"
    "v_cvt_pk_bf16_f32 v131, v198, v199\n\t"
    "v_mfma_f32_16x16x32_bf16 v[100:103], a[132:135], v[172:175], v[100:103]\n\t"
    "v_mfma_f32_16x16x32_bf16 v[100:103], a[164:167], v[188:191], v[100:103]\n\t"
    "ds_read_b128 a[104:107], v234 offset:40960\n\t"
    "ds_read_b128 a[108:111], v235 offset:40960\n\t"
    "v_max3_f32 v248, v200, v201, v202\n\t"
    "v_max3_f32 v248, v248, v203, v204\n\t"
    "v_max3_f32 v248, v248, v205, v206\n\t"
    "v_max_f32 v248, v248, v207\n\t"
    "v_cmp_lt_f32 vcc, s61, v248\n\t"
    "s_cbranch_vccnz .Lsite18_%=\n\t"
    ".Lback18_%=:\n\t"
    "v_mul_f32 v200, s53, v200\n\t"
    "v_mul_f32 v201, s53, v201\n\t"
    "v_mul_f32 v202, s53, v202\n\t"
    "v_mul_f32 v203, s53, v203\n\t"
    "v_mul_f32 v204, s53, v204\n\t"
    "v_mul_f32 v205, s53, v205\n\t"
    "v_mul_f32 v206, s53, v206\n\t"
    "v_mul_f32 v207, s53, v207\n\t"
    "v_mfma_f32_16x16x32_bf16 v[192:195], a[64:67], a[32:35], v[216:219]\n\t"
    "v_exp_f32 v200, v200\n\t"
    "v_mfma_f32_16x16x32_bf16 v[192:195], a[68:71], a[36:39], v[192:195]\n\t"
    "v_exp_f32 v201, v201\n\t"
    "v_mfma_f32_16x16x32_bf16 v[192:195], a[72:75], a[40:43], v[192:195]\n\t"
    "v_cvt_pk_bf16_f32 v132, v200, v201\n\t"
    "v_mfma_f32_16x16x32_bf16 v[192:195], a[76:79], a[44:47], v[192:195]\n\t"
    "ds_read_b64_tr_b16 a[152:153], v242 offset:16384\n\t"
    "ds_read_b64_tr_b16 a[154:155], v242 offset:20480\n\t"
    "v_mfma_f32_16x16x32_bf16 v[196:199], a[80:83], a[32:35], v[216:219]\n\t"
    "v_exp_f32 v202, v202\n\t"
    "v_mfma_f32_16x16x32_bf16 v[196:199], a[84:87], a[36:39], v[196:199]\n\t"
    "v_exp_f32 v203, v203\n\t"
    "v_mfma_f32_16x16x32_bf16 v[196:199], a[88:91], a[40:43], v[196:199]\n\t"
    "v_cvt_pk_bf16_f32 v133, v202, v203\n\t"
    "v_mfma_f32_16x16x32_bf16 v[196:199], a[92:95], a[44:47], v[196:199]\n\t"
    "buffer_load_dwordx4 v228, s[32:35], s60 offen offset:2048 nt lds\n\t"
    "v_mfma_f32_16x16x32_bf16 a[200:203], a[208:211], v[168:171], a[200:203]\n\t"
    "v_exp_f32 v204, v204\n\t"
    "v_mfma_f32_16x16x32_bf16 v[8:11], a[136:139], v[160:163], v[8:11]\n\t"
    "v_exp_f32 v205, v205\n\t"
    "v_mfma_f32_16x16x32_bf16 v[8:11], a[168:171], v[176:179], v[8:11]\n\t"
    "v_cvt_pk_bf16_f32 v134, v204, v205\n\t"
    "v_mfma_f32_16x16x32_bf16 v[40:43], a[136:139], v[164:167], v[40:43]\n\t"
    "ds_read_b64_tr_b16 a[184:185], v242 offset:24576\n\t"
    "ds_read_b64_tr_b16 a[186:187], v242 offset:28672\n\t"
    "v_mfma_f32_16x16x32_bf16 v[40:43], a[168:171], v[180:183], v[40:43]\n\t"
    "v_exp_f32 v206, v206\n\t"
    "v_mfma_f32_16x16x32_bf16 v[72:75], a[136:139], v[168:171], v[72:75]\n\t"
    "v_exp_f32 v207, v207\n\t"
    "v_mfma_f32_16x16x32_bf16 v[72:75], a[168:171], v[184:187], v[72:75]\n\t"
    "v_cvt_pk_bf16_f32 v135, v206, v207\n\t"
    "v_mfma_f32_16x16x32_bf16 v[104:107], a[136:139], v[172:175], v[104:107]\n\t"
    "buffer_load_dwordx4 v229, s[32:35], s60 offen offset:3072 nt lds\n\t"
    "v_mfma_f32_16x16x32_bf16 v[104:107], a[168:171], v[188:191], v[104:107]\n\t"
    "ds_read_b128 a[112:115], v232 offset:45056\n\t"
    "ds_read_b128 a[116:119], v233 offset:45056\n\t"
    "v_max3_f32 v248, v192, v193, v194\n\t"
    "v_max3_f32 v248, v248, v195, v196\n\t"
    "v_max3_f32 v248, v248, v197, v198\n\t"
    "v_max_f32 v248, v248, v199\n\t"
    "v_cmp_lt_f32 vcc, s61, v248\n\t"
    "s_cbranch_vccnz .Lsite19_%=\n\t"
    ".Lback19_%=:\n\t"
    "v_mul_f32 v192, s53, v192\n\t"
    "v_mul_f32 v193, s53, v193\n\t"
    "v_mul_f32 v194, s53, v194\n\t"
    "v_mul_f32 v195, s53, v195\n\t"
    "v_mul_f32 v196, s53, v196\n\t"
    "v_mul_f32 v197, s53, v197\n\t"
    "v_mul_f32 v198, s53, v198\n\t"
    "v_mul_f32 v199, s53, v199\n\t"
    "v_mfma_f32_16x16x32_bf16 v[200:203], a[64:67], a[48:51], v[220:223]\n\t"
    "v_exp_f32 v192, v192\n\t"
    "v_mfma_f32_16x16x32_bf16 v[200:203], a[68:71], a[52:55], v[200:203]\n\t"
    "v_exp_f32 v193, v193\n\t"
    "v_mfma_f32_16x16x32_bf16 v[200:203], a[72:75], a[56:59], v[200:203]\n\t"
    "v_cvt_pk_bf16_f32 v136, v192, v193\n\t"
    "v_mfma_f32_16x16x32_bf16 v[200:203], a[76:79], a[60:63], v[200:203]\n\t"
    "ds_read_b64_tr_b16 a[156:157], v243 offset:16384\n\t"
    "ds_read_b64_tr_b16 a[158:159], v243 offset:20480\n\t"
    "v_mfma_f32_16x16x32_bf16 v[204:207], a[80:83], a[48:51], v[220:223]\n\t"
    "v_exp_f32 v194, v194\n\t"
    "v_mfma_f32_16x16x32_bf16 v[204:207], a[84:87], a[52:55], v[204:207]\n\t"
    "v_exp_f32 v195, v195\n\t"
    "v_mfma_f32_16x16x32_bf16 v[204:207], a[88:91], a[56:59], v[204:207]\n\t"
    "v_cvt_pk_bf16_f32 v137, v194, v195\n\t"
    "v_mfma_f32_16x16x32_bf16 v[204:207], a[92:95], a[60:63], v[204:207]\n\t"
    "ds_read_b64_tr_b16 a[188:189], v243 offset:24576\n\t"
    "ds_read_b64_tr_b16 a[190:191], v243 offset:28672\n\t"
    "v_mfma_f32_16x16x32_bf16 a[204:207], a[208:211], v[172:175], a[204:207]\n\t"
    "v_exp_f32 v196, v196\n\t"
    "v_mfma_f32_16x16x32_bf16 v[12:15], a[140:143], v[160:163], v[12:15]\n\t"
    "v_exp_f32 v197, v197\n\t"
    "v_mfma_f32_16x16x32_bf16 v[12:15], a[172:175], v[176:179], v[12:15]\n\t"
    "v_cvt_pk_bf16_f32 v138, v196, v197\n\t"
    "v_mfma_f32_16x16x32_bf16 v[44:47], a[140:143], v[164:167], v[44:47]\n\t"
    "ds_read_b128 a[120:123], v234 offset:45056\n\t"
    "ds_read_b128 a[124:127], v235 offset:45056\n\t"
    "v_mfma_f32_16x16x32_bf16 v[44:47], a[172:175], v[180:183], v[44:47]\n\t"
    "v_exp_f32 v198, v198\n\t"
    "v_mfma_f32_16x16x32_bf16 v[76:79], a[140:143], v[168:171], v[76:79]\n\t"
    "v_exp_f32 v199, v199\n\t"
    "v_mfma_f32_16x16x32_bf16 v[76:79], a[172:175], v[184:187], v[76:79]\n\t"
    "v_cvt_pk_bf16_f32 v139, v198, v199\n\t"
    "v_mfma_f32_16x16x32_bf16 v[108:111], a[140:143], v[172:175], v[108:111]\n\t"
    "s_add_u32 s60, s60, 0x4000\n\t"
    "v_mfma_f32_16x16x32_bf16 v[108:111], a[172:175], v[188:191], v[108:111]\n\t"
    "s_waitcnt vmcnt(8) lgkmcnt(0)\n\t"
    "s_barrier\n\t"
    "v_max3_f32 v248, v200, v201, v202\n\t"
    "v_max3_f32 v248, v248, v203, v204\n\t"
    "v_max3_f32 v248, v248, v205, v206\n\t"
    "v_max_f32 v248, v248, v207\n\t"
    "v_cmp_lt_f32 vcc, s61, v248\n\t"
    "s_cbranch_vccnz .Lsite20_%=\n\t"
    ".Lback20_%=:\n\t"
    "v_mul_f32 v200, s53, v200\n\t"
    "v_mul_f32 v201, s53, v201\n\t"
    "v_mul_f32 v202, s53, v202\n\t"
    "v_mul_f32 v203, s53, v203\n\t"
    "v_mul_f32 v204, s53, v204\n\t"
    "v_mul_f32 v205, s53, v205\n\t"
    "v_mul_f32 v206, s53, v206\n\t"
    "v_mul_f32 v207, s53, v207\n\t"
    "v_mfma_f32_16x16x32_bf16 v[192:195], a[96:99], a[0:3], v[208:211]\n\t"
    "v_exp_f32 v200, v200\n\t"
    "v_mfma_f32_16x16x32_bf16 v[192:195], a[100:103], a[4:7], v[192:195]\n\t"
    "v_exp_f32 v201, v201\n\t"
    "v_mfma_f32_16x16x32_bf16 v[192:195], a[104:107], a[8:11], v[192:195]\n\t"
    "v_cvt_pk_bf16_f32 v140, v200, v201\n\t"
    "v_mfma_f32_16x16x32_bf16 v[192:195], a[108:111], a[12:15], v[192:195]\n\t"
    "ds_read_b64_tr_b16 a[128:129], v236 offset:32768\n\t"
    "ds_read_b64_tr_b16 a[130:131], v236 offset:36864\n\t"
    "v_mfma_f32_16x16x32_bf16 v[196:199], a[112:115], a[0:3], v[208:211]\n\t"
    "v_exp_f32 v202, v202\n\t"
    "v_mfma_f32_16x16x32_bf16 v[196:199], a[116:119], a[4:7], v[196:199]\n\t"
    "v_exp_f32 v203, v203\n\t"
    "v_mfma_f32_16x16x32_bf16 v[196:199], a[120:123], a[8:11], v[196:199]\n\t"
    "v_cvt_pk_bf16_f32 v141, v202, v203\n\t"
    "v_mfma_f32_16x16x32_bf16 v[196:199], a[124:127], a[12:15], v[196:199]\n\t"
    "s_add_u32 m0, s73, 0x4000\n\t"
    "v_mfma_f32_16x16x32_bf16 a[192:195], a[208:211], v[176:179], a[192:195]\n\t"
    "v_exp_f32 v204, v204\n\t"
    "v_mfma_f32_16x16x32_bf16 v[16:19], a[144:147], v[160:163], v[16:19]\n\t"
    "v_exp_f32 v205, v205\n\t"
    "v_mfma_f32_16x16x32_bf16 v[16:19], a[176:179], v[176:179], v[16:19]\n\t"
    "v_cvt_pk_bf16_f32 v142, v204, v205\n\t"
    "v_mfma_f32_16x16x32_bf16 v[48:51], a[144:147], v[164:167], v[48:51]\n\t"
    "ds_read_b64_tr_b16 a[160:161], v236 offset:40960\n\t"
    "ds_read_b64_tr_b16 a[162:163], v236 offset:45056\n\t"
    "v_mfma_f32_16x16x32_bf16 v[48:51], a[176:179], v[180:183], v[48:51]\n\t"
    "v_exp_f32 v206, v206\n\t"
    "v_mfma_f32_16x16x32_bf16 v[80:83], a[144:147], v[168:171], v[80:83]\n\t"
    "v_exp_f32 v207, v207\n\t"
    "v_mfma_f32_16x16x32_bf16 v[80:83], a[176:179], v[184:187], v[80:83]\n\t"
    "v_cvt_pk_bf16_f32 v143, v206, v207\n\t"
    "v_mfma_f32_16x16x32_bf16 v[112:115], a[144:147], v[172:175], v[112:115]\n\t"
    "buffer_load_dwordx4 v224, s[28:31], s62 offen nt lds\n\t"
    "v_mfma_f32_16x16x32_bf16 v[112:115], a[176:179], v[188:191], v[112:115]\n\t"
    "ds_read_b128 a[64:67], v232 offset:49152\n\t"
    "ds_read_b128 a[68:71], v233 offset:49152\n\t"
    "v_max3_f32 v248, v192, v193, v194\n\t"
    "v_max3_f32 v248, v248, v195, v196\n\t"
    "v_max3_f32 v248, v248, v197, v198\n\t"
    "v_max_f32 v248, v248, v199\n\t"
    "v_cmp_lt_f32 vcc, s61, v248\n\t"
    "s_cbranch_vccnz .Lsite21_%=\n\t"
    ".Lback21_%=:\n\t"
    "v_mul_f32 v192, s53, v192\n\t"
    "v_mul_f32 v193, s53, v193\n\t"
    "v_mul_f32 v194, s53, v194\n\t"
    "v_mul_f32 v195, s53, v195\n\t"
    "v_mul_f32 v196, s53, v196\n\t"
    "v_mul_f32 v197, s53, v197\n\t"
    "v_mul_f32 v198, s53, v198\n\t"
    "v_mul_f32 v199, s53, v199\n\t"
    "v_mfma_f32_16x16x32_bf16 v[200:203], a[96:99], a[16:19], v[212:215]\n\t"
    "v_exp_f32 v192, v192\n\t"
    "v_mfma_f32_16x16x32_bf16 v[200:203], a[100:103], a[20:23], v[200:203]\n\t"
    "v_exp_f32 v193, v193\n\t"
    "v_mfma_f32_16x16x32_bf16 v[200:203], a[104:107], a[24:27], v[200:203]\n\t"
    "v_cvt_pk_bf16_f32 v144, v192, v193\n\t"
    "v_mfma_f32_16x16x32_bf16 v[200:203], a[108:111], a[28:31], v[200:203]\n\t"
    "ds_read_b64_tr_b16 a[132:133], v237 offset:32768\n\t"
    "ds_read_b64_tr_b16 a[134:135], v237 offset:36864\n\t"
    "v_mfma_f32_16x16x32_bf16 v[204:207], a[112:115], a[16:19], v[212:215]\n\t"
    "v_exp_f32 v194, v194\n\t"
    "v_mfma_f32_16x16x32_bf16 v[204:207], a[116:119], a[20:23], v[204:207]\n\t"
    "v_exp_f32 v195, v195\n\t"
    "v_mfma_f32_16x16x32_bf16 v[204:207], a[120:123], a[24:27], v[204:207]\n\t"
    "v_cvt_pk_bf16_f32 v145, v194, v195\n\t"
    "v_mfma_f32_16x16x32_bf16 v[204:207], a[124:127], a[28:31], v[204:207]\n\t"
    "buffer_load_dwordx4 v225, s[28:31], s62 offen offset:1024 nt lds\n\t"
    "v_mfma_f32_16x16x32_bf16 a[196:199], a[208:211], v[180:183], a[196:199]\n\t"
    "v_exp_f32 v196, v196\n\t"
    "v_mfma_f32_16x16x32_bf16 v[20:23], a[148:151], v[160:163], v[20:23]\n\t"
    "v_exp_f32 v197, v197\n\t"
    "v_mfma_f32_16x16x32_bf16 v[20:23], a[180:183], v[176:179], v[20:23]\n\t"
    "v_cvt_pk_bf16_f32 v146, v196, v197\n\t"
    "v_mfma_f32_16x16x32_bf16 v[52:55], a[148:151], v[164:167], v[52:55]\n\t"
    "ds_read_b64_tr_b16 a[164:165], v237 offset:40960\n\t"
    "ds_read_b64_tr_b16 a[166:167], v237 offset:45056\n\t"
    "v_mfma_f32_16x16x32_bf16 v[52:55], a[180:183], v[180:183], v[52:55]\n\t"
    "v_exp_f32 v198, v198\n\t"
    "v_mfma_f32_16x16x32_bf16 v[84:87], a[148:151], v[168:171], v[84:87]\n\t"
    "v_exp_f32 v199, v199\n\t"
    "v_mfma_f32_16x16x32_bf16 v[84:87], a[180:183], v[184:187], v[84:87]\n\t"
    "v_cvt_pk_bf16_f32 v147, v198, v199\n\t"
    "v_mfma_f32_16x16x32_bf16 v[116:119], a[148:151], v[172:175], v[116:119]\n\t"
    "v_mfma_f32_16x16x32_bf16 v[116:119], a[180:183], v[188:191], v[116:119]\n\t"
    "ds_read_b128 a[72:75], v234 offset:49152\n\t"
    "ds_read_b128 a[76:79], v235 offset:49152\n\t"
    "v_max3_f32 v248, v200, v201, v202\n\t"
    "v_max3_f32 v248, v248, v203, v204\n\t"
    "v_max3_f32 v248, v248, v205, v206\n\t"
    "v_max_f32 v248, v248, v207\n\t"
    "v_cmp_lt_f32 vcc, s61, v248\n\t"
    "s_cbranch_vccnz .Lsite22_%=\n\t"
    ".Lback22_%=:\n\t"
    "v_mul_f32 v200, s53, v200\n\t"
    "v_mul_f32 v201, s53, v201\n\t"
    "v_mul_f32 v202, s53, v202\n\t"
    "v_mul_f32 v203, s53, v203\n\t"
    "v_mul_f32 v204, s53, v204\n\t"
    "v_mul_f32 v205, s53, v205\n\t"
    "v_mul_f32 v206, s53, v206\n\t"
    "v_mul_f32 v207, s53, v207\n\t"
    "v_mfma_f32_16x16x32_bf16 v[192:195], a[96:99], a[32:35], v[216:219]\n\t"
    "v_exp_f32 v200, v200\n\t"
    "v_mfma_f32_16x16x32_bf16 v[192:195], a[100:103], a[36:39], v[192:195]\n\t"
    "v_exp_f32 v201, v201\n\t"
    "v_mfma_f32_16x16x32_bf16 v[192:195], a[104:107], a[40:43], v[192:195]\n\t"
    "v_cvt_pk_bf16_f32 v148, v200, v201\n\t"
    "v_mfma_f32_16x16x32_bf16 v[192:195], a[108:111], a[44:47], v[192:195]\n\t"
    "ds_read_b64_tr_b16 a[136:137], v238 offset:32768\n\t"
    "ds_read_b64_tr_b16 a[138:139], v238 offset:36864\n\t"
    "v_mfma_f32_16x16x32_bf16 v[196:199], a[112:115], a[32:35], v[216:219]\n\t"
    "v_exp_f32 v202, v202\n\t"
    "v_mfma_f32_16x16x32_bf16 v[196:199], a[116:119], a[36:39], v[196:199]\n\t"
    "v_exp_f32 v203, v203\n\t"
    "v_mfma_f32_16x16x32_bf16 v[196:199], a[120:123], a[40:43], v[196:199]\n\t"
    "v_cvt_pk_bf16_f32 v149, v202, v203\n\t"
    "v_mfma_f32_16x16x32_bf16 v[196:199], a[124:127], a[44:47], v[196:199]\n\t"
    "buffer_load_dwordx4 v226, s[28:31], s62 offen offset:2048 nt lds\n\t"
    "v_mfma_f32_16x16x32_bf16 a[200:203], a[208:211], v[184:187], a[200:203]\n\t"
    "v_exp_f32 v204, v204\n\t"
    "v_mfma_f32_16x16x32_bf16 v[24:27], a[152:155], v[160:163], v[24:27]\n\t"
    "v_exp_f32 v205, v205\n\t"
    "v_mfma_f32_16x16x32_bf16 v[24:27], a[184:187], v[176:179], v[24:27]\n\t"
    "v_cvt_pk_bf16_f32 v150, v204, v205\n\t"
    "v_mfma_f32_16x16x32_bf16 v[56:59], a[152:155], v[164:167], v[56:59]\n\t"
    "ds_read_b64_tr_b16 a[168:169], v238 offset:40960\n\t"
    "ds_read_b64_tr_b16 a[170:171], v238 offset:45056\n\t"
    "v_mfma_f32_16x16x32_bf16 v[56:59], a[184:187], v[180:183], v[56:59]\n\t"
    "v_exp_f32 v206, v206\n\t"
    "v_mfma_f32_16x16x32_bf16 v[88:91], a[152:155], v[168:171], v[88:91]\n\t"
    "v_exp_f32 v207, v207\n\t"
    "v_mfma_f32_16x16x32_bf16 v[88:91], a[184:187], v[184:187], v[88:91]\n\t"
    "v_cvt_pk_bf16_f32 v151, v206, v207\n\t"
    "v_mfma_f32_16x16x32_bf16 v[120:123], a[152:155], v[172:175], v[120:123]\n\t"
    "buffer_load_dwordx4 v227, s[28:31], s62 offen offset:3072 nt lds\n\t"
    "v_mfma_f32_16x16x32_bf16 v[120:123], a[184:187], v[188:191], v[120:123]\n\t"
    "ds_read_b128 a[80:83], v232 offset:53248\n\t"
    "ds_read_b128 a[84:87], v233 offset:53248\n\t"
    "v_max3_f32 v248, v192, v193, v194\n\t"
    "v_max3_f32 v248, v248, v195, v196\n\t"
    "v_max3_f32 v248, v248, v197, v198\n\t"
    "v_max_f32 v248, v248, v199\n\t"
    "v_cmp_lt_f32 vcc, s61, v248\n\t"
    "s_cbranch_vccnz .Lsite23_%=\n\t"
    ".Lback23_%=:\n\t"
    "v_mul_f32 v192, s53, v192\n\t"
    "v_mul_f32 v193, s53, v193\n\t"
    "v_mul_f32 v194, s53, v194\n\t"
    "v_mul_f32 v195, s53, v195\n\t"
    "v_mul_f32 v196, s53, v196\n\t"
    "v_mul_f32 v197, s53, v197\n\t"
    "v_mul_f32 v198, s53, v198\n\t"
    "v_mul_f32 v199, s53, v199\n\t"
    "v_mfma_f32_16x16x32_bf16 v[200:203], a[96:99], a[48:51], v[220:223]\n\t"
    "v_exp_f32 v192, v192\n\t"
    "v_mfma_f32_16x16x32_bf16 v[200:203], a[100:103], a[52:55], v[200:203]\n\t"
    "v_exp_f32 v193, v193\n\t"
    "v_mfma_f32_16x16x32_bf16 v[200:203], a[104:107], a[56:59], v[200:203]\n\t"
    "v_cvt_pk_bf16_f32 v152, v192, v193\n\t"
    "v_mfma_f32_16x16x32_bf16 v[200:203], a[108:111], a[60:63], v[200:203]\n\t"
    "ds_read_b64_tr_b16 a[140:141], v239 offset:32768\n\t"
    "ds_read_b64_tr_b16 a[142:143], v239 offset:36864\n\t"
    "v_mfma_f32_16x16x32_bf16 v[204:207], a[112:115], a[48:51], v[220:223]\n\t"
    "v_exp_f32 v194, v194\n\t"
    "v_mfma_f32_16x16x32_bf16 v[204:207], a[116:119], a[52:55], v[204:207]\n\t"
    "v_exp_f32 v195, v195\n\t"
    "v_mfma_f32_16x16x32_bf16 v[204:207], a[120:123], a[56:59], v[204:207]\n\t"
    "v_cvt_pk_bf16_f32 v153, v194, v195\n\t"
    "v_mfma_f32_16x16x32_bf16 v[204:207], a[124:127], a[60:63], v[204:207]\n\t"
    "ds_read_b64_tr_b16 a[172:173], v239 offset:40960\n\t"
    "ds_read_b64_tr_b16 a[174:175], v239 offset:45056\n\t"
    "v_mfma_f32_16x16x32_bf16 a[204:207], a[208:211], v[188:191], a[204:207]\n\t"
    "v_exp_f32 v196, v196\n\t"
    "v_mfma_f32_16x16x32_bf16 v[28:31], a[156:159], v[160:163], v[28:31]\n\t"
    "v_exp_f32 v197, v197\n\t"
    "v_mfma_f32_16x16x32_bf16 v[28:31], a[188:191], v[176:179], v[28:31]\n\t"
    "v_cvt_pk_bf16_f32 v154, v196, v197\n\t"
    "v_mfma_f32_16x16x32_bf16 v[60:63], a[156:159], v[164:167], v[60:63]\n\t"
    "ds_read_b128 a[88:91], v234 offset:53248\n\t"
    "ds_read_b128 a[92:95], v235 offset:53248\n\t"
    "v_mfma_f32_16x16x32_bf16 v[60:63], a[188:191], v[180:183], v[60:63]\n\t"
    "v_exp_f32 v198, v198\n\t"
    "v_mfma_f32_16x16x32_bf16 v[92:95], a[156:159], v[168:171], v[92:95]\n\t"
    "v_exp_f32 v199, v199\n\t"
    "v_mfma_f32_16x16x32_bf16 v[92:95], a[188:191], v[184:187], v[92:95]\n\t"
    "v_cvt_pk_bf16_f32 v155, v198, v199\n\t"
    "v_mfma_f32_16x16x32_bf16 v[124:127], a[156:159], v[172:175], v[124:127]\n\t"
    "s_add_u32 s62, s62, 0x4000\n\t"
    "v_mfma_f32_16x16x32_bf16 v[124:127], a[188:191], v[188:191], v[124:127]\n\t"
    "s_waitcnt lgkmcnt(0)\n\t"
    "v_max3_f32 v248, v200, v201, v202\n\t"
    "v_max3_f32 v248, v248, v203, v204\n\t"
    "v_max3_f32 v248, v248, v205, v206\n\t"
    "v_max_f32 v248, v248, v207\n\t"
    "v_cmp_lt_f32 vcc, s61, v248\n\t"
    "s_cbranch_vccnz .Lsite24_%=\n\t"
    ".Lback24_%=:\n\t"
    "v_mul_f32 v200, s53, v200\n\t"
    "v_mul_f32 v201, s53, v201\n\t"
    "v_mul_f32 v202, s53, v202\n\t"
    "v_mul_f32 v203, s53, v203\n\t"
    "v_mul_f32 v204, s53, v204\n\t"
    "v_mul_f32 v205, s53, v205\n\t"
    "v_mul_f32 v206, s53, v206\n\t"
    "v_mul_f32 v207, s53, v207\n\t"
    "v_mfma_f32_16x16x32_bf16 v[192:195], a[64:67], a[0:3], v[208:211]\n\t"
    "v_exp_f32 v200, v200\n\t"
    "v_mfma_f32_16x16x32_bf16 v[192:195], a[68:71], a[4:7], v[192:195]\n\t"
    "v_exp_f32 v201, v201\n\t"
    "v_mfma_f32_16x16x32_bf16 v[192:195], a[72:75], a[8:11], v[192:195]\n\t"
    "v_cvt_pk_bf16_f32 v156, v200, v201\n\t"
    "v_mfma_f32_16x16x32_bf16 v[192:195], a[76:79], a[12:15], v[192:195]\n\t"
    "ds_read_b64_tr_b16 a[144:145], v240 offset:32768\n\t"
    "ds_read_b64_tr_b16 a[146:147], v240 offset:36864\n\t"
    "v_mfma_f32_16x16x32_bf16 v[196:199], a[80:83], a[0:3], v[208:211]\n\t"
    "v_exp_f32 v202, v202\n\t"
    "v_mfma_f32_16x16x32_bf16 v[196:199], a[84:87], a[4:7], v[196:199]\n\t"
    "v_exp_f32 v203, v203\n\t"
    "v_mfma_f32_16x16x32_bf16 v[196:199], a[88:91], a[8:11], v[196:199]\n\t"
    "v_cvt_pk_bf16_f32 v157, v202, v203\n\t"
    "v_mfma_f32_16x16x32_bf16 v[196:199], a[92:95], a[12:15], v[196:199]\n\t"
    "s_add_u32 m0, s73, 0x10000\n\t"
    "v_mfma_f32_16x16x32_bf16 a[192:195], a[208:211], v[128:131], a[192:195]\n\t"
    "v_exp_f32 v204, v204\n\t"
    "v_mfma_f32_16x16x32_bf16 v[0:3], a[128:131], v[128:131], v[0:3]\n\t"
    "v_exp_f32 v205, v205\n\t"
    "v_mfma_f32_16x16x32_bf16 v[0:3], a[160:163], v[144:147], v[0:3]\n\t"
    "v_cvt_pk_bf16_f32 v158, v204, v205\n\t"
    "v_mfma_f32_16x16x32_bf16 v[32:35], a[128:131], v[132:135], v[32:35]\n\t"
    "ds_read_b64_tr_b16 a[176:177], v240 offset:40960\n\t"
    "ds_read_b64_tr_b16 a[178:179], v240 offset:45056\n\t"
    "v_mfma_f32_16x16x32_bf16 v[32:35], a[160:163], v[148:151], v[32:35]\n\t"
    "v_exp_f32 v206, v206\n\t"
    "v_mfma_f32_16x16x32_bf16 v[64:67], a[128:131], v[136:139], v[64:67]\n\t"
    "v_exp_f32 v207, v207\n\t"
    "v_mfma_f32_16x16x32_bf16 v[64:67], a[160:163], v[152:155], v[64:67]\n\t"
    "v_cvt_pk_bf16_f32 v159, v206, v207\n\t"
    "v_mfma_f32_16x16x32_bf16 v[96:99], a[128:131], v[140:143], v[96:99]\n\t"
    "buffer_load_dwordx4 v228, s[32:35], s60 offen nt lds\n\t"
    "v_mfma_f32_16x16x32_bf16 v[96:99], a[160:163], v[156:159], v[96:99]\n\t"
    "ds_read_b128 a[96:99], v232 offset:57344\n\t"
    "ds_read_b128 a[100:103], v233 offset:57344\n\t"
    "v_max3_f32 v248, v192, v193, v194\n\t"
    "v_max3_f32 v248, v248, v195, v196\n\t"
    "v_max3_f32 v248, v248, v197, v198\n\t"
    "v_max_f32 v248, v248, v199\n\t"
    "v_cmp_lt_f32 vcc, s61, v248\n\t"
    "s_cbranch_vccnz .Lsite25_%=\n\t"
    ".Lback25_%=:\n\t"
    "v_mul_f32 v192, s53, v192\n\t"
    "v_mul_f32 v193, s53, v193\n\t"
    "v_mul_f32 v194, s53, v194\n\t"
    "v_mul_f32 v195, s53, v195\n\t"
    "v_mul_f32 v196, s53, v196\n\t"
    "v_mul_f32 v197, s53, v197\n\t"
    "v_mul_f32 v198, s53, v198\n\t"
    "v_mul_f32 v199, s53, v199\n\t"
    "v_mfma_f32_16x16x32_bf16 v[200:203], a[64:67], a[16:19], v[212:215]\n\t"
    "v_exp_f32 v192, v192\n\t"
    "v_mfma_f32_16x16x32_bf16 v[200:203], a[68:71], a[20:23], v[200:203]\n\t"
    "v_exp_f32 v193, v193\n\t"
    "v_mfma_f32_16x16x32_bf16 v[200:203], a[72:75], a[24:27], v[200:203]\n\t"
    "v_cvt_pk_bf16_f32 v160, v192, v193\n\t"
    "v_mfma_f32_16x16x32_bf16 v[200:203], a[76:79], a[28:31], v[200:203]\n\t"
    "ds_read_b64_tr_b16 a[148:149], v241 offset:32768\n\t"
    "ds_read_b64_tr_b16 a[150:151], v241 offset:36864\n\t"
    "v_mfma_f32_16x16x32_bf16 v[204:207], a[80:83], a[16:19], v[212:215]\n\t"
    "v_exp_f32 v194, v194\n\t"
    "v_mfma_f32_16x16x32_bf16 v[204:207], a[84:87], a[20:23], v[204:207]\n\t"
    "v_exp_f32 v195, v195\n\t"
    "v_mfma_f32_16x16x32_bf16 v[204:207], a[88:91], a[24:27], v[204:207]\n\t"
    "v_cvt_pk_bf16_f32 v161, v194, v195\n\t"
    "v_mfma_f32_16x16x32_bf16 v[204:207], a[92:95], a[28:31], v[204:207]\n\t"
    "buffer_load_dwordx4 v229, s[32:35], s60 offen offset:1024 nt lds\n\t"
    "v_mfma_f32_16x16x32_bf16 a[196:199], a[208:211], v[132:135], a[196:199]\n\t"
    "v_exp_f32 v196, v196\n\t"
    "v_mfma_f32_16x16x32_bf16 v[4:7], a[132:135], v[128:131], v[4:7]\n\t"
    "v_exp_f32 v197, v197\n\t"
    "v_mfma_f32_16x16x32_bf16 v[4:7], a[164:167], v[144:147], v[4:7]\n\t"
    "v_cvt_pk_bf16_f32 v162, v196, v197\n\t"
    "v_mfma_f32_16x16x32_bf16 v[36:39], a[132:135], v[132:135], v[36:39]\n\t"
    "ds_read_b64_tr_b16 a[180:181], v241 offset:40960\n\t"
    "ds_read_b64_tr_b16 a[182:183], v241 offset:45056\n\t"
    "v_mfma_f32_16x16x32_bf16 v[36:39], a[164:167], v[148:151], v[36:39]\n\t"
    "v_exp_f32 v198, v198\n\t"
    "v_mfma_f32_16x16x32_bf16 v[68:71], a[132:135], v[136:139], v[68:71]\n\t"
    "v_exp_f32 v199, v199\n\t"
    "v_mfma_f32_16x16x32_bf16 v[68:71], a[164:167], v[152:155], v[68:71]\n\t"
    "v_cvt_pk_bf16_f32 v163, v198, v199\n\t"
    "v_mfma_f32_16x16x32_bf16 v[100:103], a[132:135], v[140:143], v[100:103]\n\t"
    "v_mfma_f32_16x16x32_bf16 v[100:103], a[164:167], v[156:159], v[100:103]\n\t"
    "ds_read_b128 a[104:107], v234 offset:57344\n\t"
    "ds_read_b128 a[108:111], v235 offset:57344\n\t"
    "v_max3_f32 v248, v200, v201, v202\n\t"
    "v_max3_f32 v248, v248, v203, v204\n\t"
    "v_max3_f32 v248, v248, v205, v206\n\t"
    "v_max_f32 v248, v248, v207\n\t"
    "v_cmp_lt_f32 vcc, s61, v248\n\t"
    "s_cbranch_vccnz .Lsite26_%=\n\t"
    ".Lback26_%=:\n\t"
    "v_mul_f32 v200, s53, v200\n\t"
    "v_mul_f32 v201, s53, v201\n\t"
    "v_mul_f32 v202, s53, v202\n\t"
    "v_mul_f32 v203, s53, v203\n\t"
    "v_mul_f32 v204, s53, v204\n\t"
    "v_mul_f32 v205, s53, v205\n\t"
    "v_mul_f32 v206, s53, v206\n\t"
    "v_mul_f32 v207, s53, v207\n\t"
    "v_mfma_f32_16x16x32_bf16 v[192:195], a[64:67], a[32:35], v[216:219]\n\t"
    "v_exp_f32 v200, v200\n\t"
    "v_mfma_f32_16x16x32_bf16 v[192:195], a[68:71], a[36:39], v[192:195]\n\t"
    "v_exp_f32 v201, v201\n\t"
    "v_mfma_f32_16x16x32_bf16 v[192:195], a[72:75], a[40:43], v[192:195]\n\t"
    "v_cvt_pk_bf16_f32 v164, v200, v201\n\t"
    "v_mfma_f32_16x16x32_bf16 v[192:195], a[76:79], a[44:47], v[192:195]\n\t"
    "ds_read_b64_tr_b16 a[152:153], v242 offset:32768\n\t"
    "ds_read_b64_tr_b16 a[154:155], v242 offset:36864\n\t"
    "v_mfma_f32_16x16x32_bf16 v[196:199], a[80:83], a[32:35], v[216:219]\n\t"
    "v_exp_f32 v202, v202\n\t"
    "v_mfma_f32_16x16x32_bf16 v[196:199], a[84:87], a[36:39], v[196:199]\n\t"
    "v_exp_f32 v203, v203\n\t"
    "v_mfma_f32_16x16x32_bf16 v[196:199], a[88:91], a[40:43], v[196:199]\n\t"
    "v_cvt_pk_bf16_f32 v165, v202, v203\n\t"
    "v_mfma_f32_16x16x32_bf16 v[196:199], a[92:95], a[44:47], v[196:199]\n\t"
    "buffer_load_dwordx4 v228, s[32:35], s60 offen offset:2048 nt lds\n\t"
    "v_mfma_f32_16x16x32_bf16 a[200:203], a[208:211], v[136:139], a[200:203]\n\t"
    "v_exp_f32 v204, v204\n\t"
    "v_mfma_f32_16x16x32_bf16 v[8:11], a[136:139], v[128:131], v[8:11]\n\t"
    "v_exp_f32 v205, v205\n\t"
    "v_mfma_f32_16x16x32_bf16 v[8:11], a[168:171], v[144:147], v[8:11]\n\t"
    "v_cvt_pk_bf16_f32 v166, v204, v205\n\t"
    "v_mfma_f32_16x16x32_bf16 v[40:43], a[136:139], v[132:135], v[40:43]\n\t"
    "ds_read_b64_tr_b16 a[184:185], v242 offset:40960\n\t"
    "ds_read_b64_tr_b16 a[186:187], v242 offset:45056\n\t"
    "v_mfma_f32_16x16x32_bf16 v[40:43], a[168:171], v[148:151], v[40:43]\n\t"
    "v_exp_f32 v206, v206\n\t"
    "v_mfma_f32_16x16x32_bf16 v[72:75], a[136:139], v[136:139], v[72:75]\n\t"
    "v_exp_f32 v207, v207\n\t"
    "v_mfma_f32_16x16x32_bf16 v[72:75], a[168:171], v[152:155], v[72:75]\n\t"
    "v_cvt_pk_bf16_f32 v167, v206, v207\n\t"
    "v_mfma_f32_16x16x32_bf16 v[104:107], a[136:139], v[140:143], v[104:107]\n\t"
    "buffer_load_dwordx4 v229, s[32:35], s60 offen offset:3072 nt lds\n\t"
    "v_mfma_f32_16x16x32_bf16 v[104:107], a[168:171], v[156:159], v[104:107]\n\t"
    "ds_read_b128 a[112:115], v232 offset:61440\n\t"
    "ds_read_b128 a[116:119], v233 offset:61440\n\t"
    "v_max3_f32 v248, v192, v193, v194\n\t"
    "v_max3_f32 v248, v248, v195, v196\n\t"
    "v_max3_f32 v248, v248, v197, v198\n\t"
    "v_max_f32 v248, v248, v199\n\t"
    "v_cmp_lt_f32 vcc, s61, v248\n\t"
    "s_cbranch_vccnz .Lsite27_%=\n\t"
    ".Lback27_%=:\n\t"
    "v_mul_f32 v192, s53, v192\n\t"
    "v_mul_f32 v193, s53, v193\n\t"
    "v_mul_f32 v194, s53, v194\n\t"
    "v_mul_f32 v195, s53, v195\n\t"
    "v_mul_f32 v196, s53, v196\n\t"
    "v_mul_f32 v197, s53, v197\n\t"
    "v_mul_f32 v198, s53, v198\n\t"
    "v_mul_f32 v199, s53, v199\n\t"
    "v_mfma_f32_16x16x32_bf16 v[200:203], a[64:67], a[48:51], v[220:223]\n\t"
    "v_exp_f32 v192, v192\n\t"
    "v_mfma_f32_16x16x32_bf16 v[200:203], a[68:71], a[52:55], v[200:203]\n\t"
    "v_exp_f32 v193, v193\n\t"
    "v_mfma_f32_16x16x32_bf16 v[200:203], a[72:75], a[56:59], v[200:203]\n\t"
    "v_cvt_pk_bf16_f32 v168, v192, v193\n\t"
    "v_mfma_f32_16x16x32_bf16 v[200:203], a[76:79], a[60:63], v[200:203]\n\t"
    "ds_read_b64_tr_b16 a[156:157], v243 offset:32768\n\t"
    "ds_read_b64_tr_b16 a[158:159], v243 offset:36864\n\t"
    "v_mfma_f32_16x16x32_bf16 v[204:207], a[80:83], a[48:51], v[220:223]\n\t"
    "v_exp_f32 v194, v194\n\t"
    "v_mfma_f32_16x16x32_bf16 v[204:207], a[84:87], a[52:55], v[204:207]\n\t"
    "v_exp_f32 v195, v195\n\t"
    "v_mfma_f32_16x16x32_bf16 v[204:207], a[88:91], a[56:59], v[204:207]\n\t"
    "v_cvt_pk_bf16_f32 v169, v194, v195\n\t"
    "v_mfma_f32_16x16x32_bf16 v[204:207], a[92:95], a[60:63], v[204:207]\n\t"
    "ds_read_b64_tr_b16 a[188:189], v243 offset:40960\n\t"
    "ds_read_b64_tr_b16 a[190:191], v243 offset:45056\n\t"
    "v_mfma_f32_16x16x32_bf16 a[204:207], a[208:211], v[140:143], a[204:207]\n\t"
    "v_exp_f32 v196, v196\n\t"
    "v_mfma_f32_16x16x32_bf16 v[12:15], a[140:143], v[128:131], v[12:15]\n\t"
    "v_exp_f32 v197, v197\n\t"
    "v_mfma_f32_16x16x32_bf16 v[12:15], a[172:175], v[144:147], v[12:15]\n\t"
    "v_cvt_pk_bf16_f32 v170, v196, v197\n\t"
    "v_mfma_f32_16x16x32_bf16 v[44:47], a[140:143], v[132:135], v[44:47]\n\t"
    "ds_read_b128 a[120:123], v234 offset:61440\n\t"
    "ds_read_b128 a[124:127], v235 offset:61440\n\t"
    "v_mfma_f32_16x16x32_bf16 v[44:47], a[172:175], v[148:151], v[44:47]\n\t"
    "v_exp_f32 v198, v198\n\t"
    "v_mfma_f32_16x16x32_bf16 v[76:79], a[140:143], v[136:139], v[76:79]\n\t"
    "v_exp_f32 v199, v199\n\t"
    "v_mfma_f32_16x16x32_bf16 v[76:79], a[172:175], v[152:155], v[76:79]\n\t"
    "v_cvt_pk_bf16_f32 v171, v198, v199\n\t"
    "v_mfma_f32_16x16x32_bf16 v[108:111], a[140:143], v[140:143], v[108:111]\n\t"
    "s_add_u32 s60, s60, 0x4000\n\t"
    "v_mfma_f32_16x16x32_bf16 v[108:111], a[172:175], v[156:159], v[108:111]\n\t"
    "s_waitcnt vmcnt(8) lgkmcnt(0)\n\t"
    "s_barrier\n\t"
    "v_max3_f32 v248, v200, v201, v202\n\t"
    "v_max3_f32 v248, v248, v203, v204\n\t"
    "v_max3_f32 v248, v248, v205, v206\n\t"
    "v_max_f32 v248, v248, v207\n\t"
    "v_cmp_lt_f32 vcc, s61, v248\n\t"
    "s_cbranch_vccnz .Lsite28_%=\n\t"
    ".Lback28_%=:\n\t"
    "v_mul_f32 v200, s53, v200\n\t"
    "v_mul_f32 v201, s53, v201\n\t"
    "v_mul_f32 v202, s53, v202\n\t"
    "v_mul_f32 v203, s53, v203\n\t"
    "v_mul_f32 v204, s53, v204\n\t"
    "v_mul_f32 v205, s53, v205\n\t"
    "v_mul_f32 v206, s53, v206\n\t"
    "v_mul_f32 v207, s53, v207\n\t"
    "v_mfma_f32_16x16x32_bf16 v[192:195], a[96:99], a[0:3], v[208:211]\n\t"
    "v_exp_f32 v200, v200\n\t"
    "v_mfma_f32_16x16x32_bf16 v[192:195], a[100:103], a[4:7], v[192:195]\n\t"
    "v_exp_f32 v201, v201\n\t"
    "v_mfma_f32_16x16x32_bf16 v[192:195], a[104:107], a[8:11], v[192:195]\n\t"
    "v_cvt_pk_bf16_f32 v172, v200, v201\n\t"
    "v_mfma_f32_16x16x32_bf16 v[192:195], a[108:111], a[12:15], v[192:195]\n\t"
    "ds_read_b64_tr_b16 a[128:129], v236 offset:49152\n\t"
    "ds_read_b64_tr_b16 a[130:131], v236 offset:53248\n\t"
    "v_mfma_f32_16x16x32_bf16 v[196:199], a[112:115], a[0:3], v[208:211]\n\t"
    "v_exp_f32 v202, v202\n\t"
    "v_mfma_f32_16x16x32_bf16 v[196:199], a[116:119], a[4:7], v[196:199]\n\t"
    "v_exp_f32 v203, v203\n\t"
    "v_mfma_f32_16x16x32_bf16 v[196:199], a[120:123], a[8:11], v[196:199]\n\t"
    "v_cvt_pk_bf16_f32 v173, v202, v203\n\t"
    "v_mfma_f32_16x16x32_bf16 v[196:199], a[124:127], a[12:15], v[196:199]\n\t"
    "s_add_u32 m0, s73, 0x8000\n\t"
    "v_mfma_f32_16x16x32_bf16 a[192:195], a[208:211], v[144:147], a[192:195]\n\t"
    "v_exp_f32 v204, v204\n\t"
    "v_mfma_f32_16x16x32_bf16 v[16:19], a[144:147], v[128:131], v[16:19]\n\t"
    "v_exp_f32 v205, v205\n\t"
    "v_mfma_f32_16x16x32_bf16 v[16:19], a[176:179], v[144:147], v[16:19]\n\t"
    "v_cvt_pk_bf16_f32 v174, v204, v205\n\t"
    "v_mfma_f32_16x16x32_bf16 v[48:51], a[144:147], v[132:135], v[48:51]\n\t"
    "ds_read_b64_tr_b16 a[160:161], v236 offset:57344\n\t"
    "ds_read_b64_tr_b16 a[162:163], v236 offset:61440\n\t"
    "v_mfma_f32_16x16x32_bf16 v[48:51], a[176:179], v[148:151], v[48:51]\n\t"
    "v_exp_f32 v206, v206\n\t"
    "v_mfma_f32_16x16x32_bf16 v[80:83], a[144:147], v[136:139], v[80:83]\n\t"
    "v_exp_f32 v207, v207\n\t"
    "v_mfma_f32_16x16x32_bf16 v[80:83], a[176:179], v[152:155], v[80:83]\n\t"
    "v_cvt_pk_bf16_f32 v175, v206, v207\n\t"
    "v_mfma_f32_16x16x32_bf16 v[112:115], a[144:147], v[140:143], v[112:115]\n\t"
    "buffer_load_dwordx4 v224, s[28:31], s62 offen nt lds\n\t"
    "v_mfma_f32_16x16x32_bf16 v[112:115], a[176:179], v[156:159], v[112:115]\n\t"
    "ds_read_b128 a[64:67], v232 offset:0\n\t"
    "ds_read_b128 a[68:71], v233 offset:0\n\t"
    "v_max3_f32 v248, v192, v193, v194\n\t"
    "v_max3_f32 v248, v248, v195, v196\n\t"
    "v_max3_f32 v248, v248, v197, v198\n\t"
    "v_max_f32 v248, v248, v199\n\t"
    "v_cmp_lt_f32 vcc, s61, v248\n\t"
    "s_cbranch_vccnz .Lsite29_%=\n\t"
    ".Lback29_%=:\n\t"
    "v_mul_f32 v192, s53, v192\n\t"
    "v_mul_f32 v193, s53, v193\n\t"
    "v_mul_f32 v194, s53, v194\n\t"
    "v_mul_f32 v195, s53, v195\n\t"
    "v_mul_f32 v196, s53, v196\n\t"
    "v_mul_f32 v197, s53, v197\n\t"
    "v_mul_f32 v198, s53, v198\n\t"
    "v_mul_f32 v199, s53, v199\n\t"
    "v_mfma_f32_16x16x32_bf16 v[200:203], a[96:99], a[16:19], v[212:215]\n\t"
    "v_exp_f32 v192, v192\n\t"
    "v_mfma_f32_16x16x32_bf16 v[200:203], a[100:103], a[20:23], v[200:203]\n\t"
    "v_exp_f32 v193, v193\n\t"
    "v_mfma_f32_16x16x32_bf16 v[200:203], a[104:107], a[24:27], v[200:203]\n\t"
    "v_cvt_pk_bf16_f32 v176, v192, v193\n\t"
    "v_mfma_f32_16x16x32_bf16 v[200:203], a[108:111], a[28:31], v[200:203]\n\t"
    "ds_read_b64_tr_b16 a[132:133], v237 offset:49152\n\t"
    "ds_read_b64_tr_b16 a[134:135], v237 offset:53248\n\t"
    "v_mfma_f32_16x16x32_bf16 v[204:207], a[112:115], a[16:19], v[212:215]\n\t"
    "v_exp_f32 v194, v194\n\t"
    "v_mfma_f32_16x16x32_bf16 v[204:207], a[116:119], a[20:23], v[204:207]\n\t"
    "v_exp_f32 v195, v195\n\t"
    "v_mfma_f32_16x16x32_bf16 v[204:207], a[120:123], a[24:27], v[204:207]\n\t"
    "v_cvt_pk_bf16_f32 v177, v194, v195\n\t"
    "v_mfma_f32_16x16x32_bf16 v[204:207], a[124:127], a[28:31], v[204:207]\n\t"
    "buffer_load_dwordx4 v225, s[28:31], s62 offen offset:1024 nt lds\n\t"
    "v_mfma_f32_16x16x32_bf16 a[196:199], a[208:211], v[148:151], a[196:199]\n\t"
    "v_exp_f32 v196, v196\n\t"
    "v_mfma_f32_16x16x32_bf16 v[20:23], a[148:151], v[128:131], v[20:23]\n\t"
    "v_exp_f32 v197, v197\n\t"
    "v_mfma_f32_16x16x32_bf16 v[20:23], a[180:183], v[144:147], v[20:23]\n\t"
    "v_cvt_pk_bf16_f32 v178, v196, v197\n\t"
    "v_mfma_f32_16x16x32_bf16 v[52:55], a[148:151], v[132:135], v[52:55]\n\t"
    "ds_read_b64_tr_b16 a[164:165], v237 offset:57344\n\t"
    "ds_read_b64_tr_b16 a[166:167], v237 offset:61440\n\t"
    "v_mfma_f32_16x16x32_bf16 v[52:55], a[180:183], v[148:151], v[52:55]\n\t"
    "v_exp_f32 v198, v198\n\t"
    "v_mfma_f32_16x16x32_bf16 v[84:87], a[148:151], v[136:139], v[84:87]\n\t"
    "v_exp_f32 v199, v199\n\t"
    "v_mfma_f32_16x16x32_bf16 v[84:87], a[180:183], v[152:155], v[84:87]\n\t"
    "v_cvt_pk_bf16_f32 v179, v198, v199\n\t"
    "v_mfma_f32_16x16x32_bf16 v[116:119], a[148:151], v[140:143], v[116:119]\n\t"
    "v_mfma_f32_16x16x32_bf16 v[116:119], a[180:183], v[156:159], v[116:119]\n\t"
    "ds_read_b128 a[72:75], v234 offset:0\n\t"
    "ds_read_b128 a[76:79], v235 offset:0\n\t"
    "v_max3_f32 v248, v200, v201, v202\n\t"
    "v_max3_f32 v248, v248, v203, v204\n\t"
    "v_max3_f32 v248, v248, v205, v206\n\t"
    "v_max_f32 v248, v248, v207\n\t"
    "v_cmp_lt_f32 vcc, s61, v248\n\t"
    "s_cbranch_vccnz .Lsite30_%=\n\t"
    ".Lback30_%=:\n\t"
    "v_mul_f32 v200, s53, v200\n\t"
    "v_mul_f32 v201, s53, v201\n\t"
    "v_mul_f32 v202, s53, v202\n\t"
    "v_mul_f32 v203, s53, v203\n\t"
    "v_mul_f32 v204, s53, v204\n\t"
    "v_mul_f32 v205, s53, v205\n\t"
    "v_mul_f32 v206, s53, v206\n\t"
    "v_mul_f32 v207, s53, v207\n\t"
    "v_mfma_f32_16x16x32_bf16 v[192:195], a[96:99], a[32:35], v[216:219]\n\t"
    "v_exp_f32 v200, v200\n\t"
    "v_mfma_f32_16x16x32_bf16 v[192:195], a[100:103], a[36:39], v[192:195]\n\t"
    "v_exp_f32 v201, v201\n\t"
    "v_mfma_f32_16x16x32_bf16 v[192:195], a[104:107], a[40:43], v[192:195]\n\t"
    "v_cvt_pk_bf16_f32 v180, v200, v201\n\t"
    "v_mfma_f32_16x16x32_bf16 v[192:195], a[108:111], a[44:47], v[192:195]\n\t"
    "ds_read_b64_tr_b16 a[136:137], v238 offset:49152\n\t"
    "ds_read_b64_tr_b16 a[138:139], v238 offset:53248\n\t"
    "v_mfma_f32_16x16x32_bf16 v[196:199], a[112:115], a[32:35], v[216:219]\n\t"
    "v_exp_f32 v202, v202\n\t"
    "v_mfma_f32_16x16x32_bf16 v[196:199], a[116:119], a[36:39], v[196:199]\n\t"
    "v_exp_f32 v203, v203\n\t"
    "v_mfma_f32_16x16x32_bf16 v[196:199], a[120:123], a[40:43], v[196:199]\n\t"
    "v_cvt_pk_bf16_f32 v181, v202, v203\n\t"
    "v_mfma_f32_16x16x32_bf16 v[196:199], a[124:127], a[44:47], v[196:199]\n\t"
    "buffer_load_dwordx4 v226, s[28:31], s62 offen offset:2048 nt lds\n\t"
    "v_mfma_f32_16x16x32_bf16 a[200:203], a[208:211], v[152:155], a[200:203]\n\t"
    "v_exp_f32 v204, v204\n\t"
    "v_mfma_f32_16x16x32_bf16 v[24:27], a[152:155], v[128:131], v[24:27]\n\t"
    "v_exp_f32 v205, v205\n\t"
    "v_mfma_f32_16x16x32_bf16 v[24:27], a[184:187], v[144:147], v[24:27]\n\t"
    "v_cvt_pk_bf16_f32 v182, v204, v205\n\t"
    "v_mfma_f32_16x16x32_bf16 v[56:59], a[152:155], v[132:135], v[56:59]\n\t"
    "ds_read_b64_tr_b16 a[168:169], v238 offset:57344\n\t"
    "ds_read_b64_tr_b16 a[170:171], v238 offset:61440\n\t"
    "v_mfma_f32_16x16x32_bf16 v[56:59], a[184:187], v[148:151], v[56:59]\n\t"
    "v_exp_f32 v206, v206\n\t"
    "v_mfma_f32_16x16x32_bf16 v[88:91], a[152:155], v[136:139], v[88:91]\n\t"
    "v_exp_f32 v207, v207\n\t"
    "v_mfma_f32_16x16x32_bf16 v[88:91], a[184:187], v[152:155], v[88:91]\n\t"
    "v_cvt_pk_bf16_f32 v183, v206, v207\n\t"
    "v_mfma_f32_16x16x32_bf16 v[120:123], a[152:155], v[140:143], v[120:123]\n\t"
    "buffer_load_dwordx4 v227, s[28:31], s62 offen offset:3072 nt lds\n\t"
    "v_mfma_f32_16x16x32_bf16 v[120:123], a[184:187], v[156:159], v[120:123]\n\t"
    "ds_read_b128 a[80:83], v232 offset:4096\n\t"
    "ds_read_b128 a[84:87], v233 offset:4096\n\t"
    "v_max3_f32 v248, v192, v193, v194\n\t"
    "v_max3_f32 v248, v248, v195, v196\n\t"
    "v_max3_f32 v248, v248, v197, v198\n\t"
    "v_max_f32 v248, v248, v199\n\t"
    "v_cmp_lt_f32 vcc, s61, v248\n\t"
    "s_cbranch_vccnz .Lsite31_%=\n\t"
    ".Lback31_%=:\n\t"
    "v_mul_f32 v192, s53, v192\n\t"
    "v_mul_f32 v193, s53, v193\n\t"
    "v_mul_f32 v194, s53, v194\n\t"
    "v_mul_f32 v195, s53, v195\n\t"
    "v_mul_f32 v196, s53, v196\n\t"
    "v_mul_f32 v197, s53, v197\n\t"
    "v_mul_f32 v198, s53, v198\n\t"
    "v_mul_f32 v199, s53, v199\n\t"
    "v_mfma_f32_16x16x32_bf16 v[200:203], a[96:99], a[48:51], v[220:223]\n\t"
    "v_exp_f32 v192, v192\n\t"
    "v_mfma_f32_16x16x32_bf16 v[200:203], a[100:103], a[52:55], v[200:203]\n\t"
    "v_exp_f32 v193, v193\n\t"
    "v_mfma_f32_16x16x32_bf16 v[200:203], a[104:107], a[56:59], v[200:203]\n\t"
    "v_cvt_pk_bf16_f32 v184, v192, v193\n\t"
    "v_mfma_f32_16x16x32_bf16 v[200:203], a[108:111], a[60:63], v[200:203]\n\t"
    "ds_read_b64_tr_b16 a[140:141], v239 offset:49152\n\t"
    "ds_read_b64_tr_b16 a[142:143], v239 offset:53248\n\t"
    "v_mfma_f32_16x16x32_bf16 v[204:207], a[112:115], a[48:51], v[220:223]\n\t"
    "v_exp_f32 v194, v194\n\t"
    "v_mfma_f32_16x16x32_bf16 v[204:207], a[116:119], a[52:55], v[204:207]\n\t"
    "v_exp_f32 v195, v195\n\t"
    "v_mfma_f32_16x16x32_bf16 v[204:207], a[120:123], a[56:59], v[204:207]\n\t"
    "v_cvt_pk_bf16_f32 v185, v194, v195\n\t"
    "v_mfma_f32_16x16x32_bf16 v[204:207], a[124:127], a[60:63], v[204:207]\n\t"
    "ds_read_b64_tr_b16 a[172:173], v239 offset:57344\n\t"
    "ds_read_b64_tr_b16 a[174:175], v239 offset:61440\n\t"
    "v_mfma_f32_16x16x32_bf16 a[204:207], a[208:211], v[156:159], a[204:207]\n\t"
    "v_exp_f32 v196, v196\n\t"
    "v_mfma_f32_16x16x32_bf16 v[28:31], a[156:159], v[128:131], v[28:31]\n\t"
    "v_exp_f32 v197, v197\n\t"
    "v_mfma_f32_16x16x32_bf16 v[28:31], a[188:191], v[144:147], v[28:31]\n\t"
    "v_cvt_pk_bf16_f32 v186, v196, v197\n\t"
    "v_mfma_f32_16x16x32_bf16 v[60:63], a[156:159], v[132:135], v[60:63]\n\t"
    "ds_read_b128 a[88:91], v234 offset:4096\n\t"
    "ds_read_b128 a[92:95], v235 offset:4096\n\t"
    "v_mfma_f32_16x16x32_bf16 v[60:63], a[188:191], v[148:151], v[60:63]\n\t"
    "v_exp_f32 v198, v198\n\t"
    "v_mfma_f32_16x16x32_bf16 v[92:95], a[156:159], v[136:139], v[92:95]\n\t"
    "v_exp_f32 v199, v199\n\t"
    "v_mfma_f32_16x16x32_bf16 v[92:95], a[188:191], v[152:155], v[92:95]\n\t"
    "v_cvt_pk_bf16_f32 v187, v198, v199\n\t"
    "v_mfma_f32_16x16x32_bf16 v[124:127], a[156:159], v[140:143], v[124:127]\n\t"
    "s_add_u32 s62, s62, 0x4000\n\t"
    "v_mfma_f32_16x16x32_bf16 v[124:127], a[188:191], v[156:159], v[124:127]\n\t"
    "s_cmp_lg_u32 s60, 0x44000\n\t"
    "s_waitcnt lgkmcnt(0)\n\t"
    "s_cbranch_scc1 .Lsloop%=\n\t"
    "v_max3_f32 v248, v200, v201, v202\n\t"
    "v_max3_f32 v248, v248, v203, v204\n\t"
    "v_max3_f32 v248, v248, v205, v206\n\t"
    "v_max_f32 v248, v248, v207\n\t"
    "v_cmp_lt_f32 vcc, s61, v248\n\t"
    "s_cbranch_vccnz .Lsite32_%=\n\t"
    ".Lback32_%=:\n\t"
    "v_mul_f32 v200, s53, v200\n\t"
    "v_mul_f32 v201, s53, v201\n\t"
    "v_mul_f32 v202, s53, v202\n\t"
    "v_mul_f32 v203, s53, v203\n\t"
    "v_mul_f32 v204, s53, v204\n\t"
    "v_mul_f32 v205, s53, v205\n\t"
    "v_mul_f32 v206, s53, v206\n\t"
    "v_mul_f32 v207, s53, v207\n\t"
    "s_branch .Ldrain%=\n\t"
    ".Lsite0_%=:\n\t"
    "s_call_b64 s[90:91], .Lrare3_%=\n\t"
    "s_branch .Lback0_%=\n\t"
    ".Lsite1_%=:\n\t"
    "s_call_b64 s[90:91], .Lrare0_%=\n\t"
    "s_branch .Lback1_%=\n\t"
    ".Lsite2_%=:\n\t"
    "s_call_b64 s[90:91], .Lrare1_%=\n\t"
    "s_branch .Lback2_%=\n\t"
    ".Lsite3_%=:\n\t"
    "s_call_b64 s[90:91], .Lrare2_%=\n\t"
    "s_branch .Lback3_%=\n\t"
    ".Lsite4_%=:\n\t"
    "s_call_b64 s[90:91], .Lrare3_%=\n\t"
    "s_branch .Lback4_%=\n\t"
    ".Lsite5_%=:\n\t"
    "s_call_b64 s[90:91], .Lrare0_%=\n\t"
    "s_branch .Lback5_%=\n\t"
    ".Lsite6_%=:\n\t"
    "s_call_b64 s[90:91], .Lrare1_%=\n\t"
    "s_branch .Lback6_%=\n\t"
    ".Lsite7_%=:\n\t"
    "s_call_b64 s[90:91], .Lrare2_%=\n\t"
    "s_branch .Lback7_%=\n\t"
    ".Lsite8_%=:\n\t"
    "s_call_b64 s[90:91], .Lrare3_%=\n\t"
    "s_branch .Lback8_%=\n\t"
    ".Lsite9_%=:\n\t"
    "s_call_b64 s[90:91], .Lrare0_%=\n\t"
    "s_branch .Lback9_%=\n\t"
    ".Lsite10_%=:\n\t"
    "s_call_b64 s[90:91], .Lrare1_%=\n\t"
    "s_branch .Lback10_%=\n\t"
    ".Lsite11_%=:\n\t"
    "s_call_b64 s[90:91], .Lrare2_%=\n\t"
    "s_branch .Lback11_%=\n\t"
    ".Lsite12_%=:\n\t"
    "s_call_b64 s[90:91], .Lrare3_%=\n\t"
    "s_branch .Lback12_%=\n\t"
    ".Lsite13_%=:\n\t"
    "s_call_b64 s[90:91], .Lrare0_%=\n\t"
    "s_branch .Lback13_%=\n\t"
    ".Lsite14_%=:\n\t"
    "s_call_b64 s[90:91], .Lrare1_%=\n\t"
    "s_branch .Lback14_%=\n\t"
    ".Lsite15_%=:\n\t"
    "s_call_b64 s[90:91], .Lrare2_%=\n\t"
    "s_branch .Lback15_%=\n\t"
    ".Lsite16_%=:\n\t"
    "s_call_b64 s[90:91], .Lrare3_%=\n\t"
    "s_branch .Lback16_%=\n\t"
    ".Lsite17_%=:\n\t"
    "s_call_b64 s[90:91], .Lrare0_%=\n\t"
    "s_branch .Lback17_%=\n\t"
    ".Lsite18_%=:\n\t"
    "s_call_b64 s[90:91], .Lrare1_%=\n\t"
    "s_branch .Lback18_%=\n\t"
    ".Lsite19_%=:\n\t"
    "s_call_b64 s[90:91], .Lrare2_%=\n\t"
    "s_branch .Lback19_%=\n\t"
    ".Lsite20_%=:\n\t"
    "s_call_b64 s[90:91], .Lrare3_%=\n\t"
    "s_branch .Lback20_%=\n\t"
    ".Lsite21_%=:\n\t"
    "s_call_b64 s[90:91], .Lrare0_%=\n\t"
    "s_branch .Lback21_%=\n\t"
    ".Lsite22_%=:\n\t"
    "s_call_b64 s[90:91], .Lrare1_%=\n\t"
    "s_branch .Lback22_%=\n\t"
    ".Lsite23_%=:\n\t"
    "s_call_b64 s[90:91], .Lrare2_%=\n\t"
    "s_branch .Lback23_%=\n\t"
    ".Lsite24_%=:\n\t"
    "s_call_b64 s[90:91], .Lrare3_%=\n\t"
    "s_branch .Lback24_%=\n\t"
    ".Lsite25_%=:\n\t"
    "s_call_b64 s[90:91], .Lrare0_%=\n\t"
    "s_branch .Lback25_%=\n\t"
    ".Lsite26_%=:\n\t"
    "s_call_b64 s[90:91], .Lrare1_%=\n\t"
    "s_branch .Lback26_%=\n\t"
    ".Lsite27_%=:\n\t"
    "s_call_b64 s[90:91], .Lrare2_%=\n\t"
    "s_branch .Lback27_%=\n\t"
    ".Lsite28_%=:\n\t"
    "s_call_b64 s[90:91], .Lrare3_%=\n\t"
    "s_branch .Lback28_%=\n\t"
    ".Lsite29_%=:\n\t"
    "s_call_b64 s[90:91], .Lrare0_%=\n\t"
    "s_branch .Lback29_%=\n\t"
    ".Lsite30_%=:\n\t"
    "s_call_b64 s[90:91], .Lrare1_%=\n\t"
    "s_branch .Lback30_%=\n\t"
    ".Lsite31_%=:\n\t"
    "s_call_b64 s[90:91], .Lrare2_%=\n\t"
    "s_branch .Lback31_%=\n\t"
    ".Lsite32_%=:\n\t"
    "s_call_b64 s[90:91], .Lrare3_%=\n\t"
    "s_branch .Lback32_%=\n\t"
    ".Lrare0_%=:\n\t"
    "s_nop 7\n\t"
    "v_mov_b32 v250, v248\n\t"
    "s_nop 1\n\t"
    "v_permlane32_swap_b32 v248, v250\n\t"
    "s_nop 1\n\t"
    "v_max_f32 v248, v248, v250\n\t"
    "v_mov_b32 v250, v248\n\t"
    "s_nop 1\n\t"
    "v_permlane16_swap_b32 v248, v250\n\t"
    "s_nop 1\n\t"
    "v_max_f32 v248, v248, v250\n\t"
    "v_max_f32 v249, 0, v248\n\t"
    "v_mul_f32 v249, s53, v249\n\t"
    "v_ceil_f32 v249, v249\n\t"
    "v_exp_f32 v250, -v249\n\t"
    "v_mul_f32 v249, 0x3f7af233, v249\n\t"
    "v_sub_f32 v208, v208, v249\n\t"
    "v_sub_f32 v209, v209, v249\n\t"
    "v_sub_f32 v210, v210, v249\n\t"
    "v_sub_f32 v211, v211, v249\n\t"
    "v_sub_f32 v192, v192, v249\n\t"
    "v_sub_f32 v193, v193, v249\n\t"
    "v_sub_f32 v194, v194, v249\n\t"
    "v_sub_f32 v195, v195, v249\n\t"
    "v_sub_f32 v196, v196, v249\n\t"
    "v_sub_f32 v197, v197, v249\n\t"
    "v_sub_f32 v198, v198, v249\n\t"
    "v_sub_f32 v199, v199, v249\n\t"
    "v_mul_f32 v0, v0, v250\n\t"
    "v_mul_f32 v1, v1, v250\n\t"
    "v_mul_f32 v2, v2, v250\n\t"
    "v_mul_f32 v3, v3, v250\n\t"
    "v_mul_f32 v4, v4, v250\n\t"
    "v_mul_f32 v5, v5, v250\n\t"
    "v_mul_f32 v6, v6, v250\n\t"
    "v_mul_f32 v7, v7, v250\n\t"
    "v_mul_f32 v8, v8, v250\n\t"
    "v_mul_f32 v9, v9, v250\n\t"
    "v_mul_f32 v10, v10, v250\n\t"
    "v_mul_f32 v11, v11, v250\n\t"
    "v_mul_f32 v12, v12, v250\n\t"
    "v_mul_f32 v13, v13, v250\n\t"
    "v_mul_f32 v14, v14, v250\n\t"
    "v_mul_f32 v15, v15, v250\n\t"
    "v_mul_f32 v16, v16, v250\n\t"
    "v_mul_f32 v17, v17, v250\n\t"
    "v_mul_f32 v18, v18, v250\n\t"
    "v_mul_f32 v19, v19, v250\n\t"
    "v_mul_f32 v20, v20, v250\n\t"
    "v_mul_f32 v21, v21, v250\n\t"
    "v_mul_f32 v22, v22, v250\n\t"
    "v_mul_f32 v23, v23, v250\n\t"
    "v_mul_f32 v24, v24, v250\n\t"
    "v_mul_f32 v25, v25, v250\n\t"
    "v_mul_f32 v26, v26, v250\n\t"
    "v_mul_f32 v27, v27, v250\n\t"
    "v_mul_f32 v28, v28, v250\n\t"
    "v_mul_f32 v29, v29, v250\n\t"
    "v_mul_f32 v30, v30, v250\n\t"
    "v_mul_f32 v31, v31, v250\n\t"
    "v_lshlrev_b32 v249, 16, v128\n\t"
    "v_and_b32 v251, 0xffff0000, v128\n\t"
    "v_mul_f32 v249, v249, v250\n\t"
    "v_mul_f32 v251, v251, v250\n\t"
    "v_cvt_pk_bf16_f32 v128, v249, v251\n\t"
    "v_lshlrev_b32 v249, 16, v129\n\t"
    "v_and_b32 v251, 0xffff0000, v129\n\t"
    "v_mul_f32 v249, v249, v250\n\t"
    "v_mul_f32 v251, v251, v250\n\t"
    "v_cvt_pk_bf16_f32 v129, v249, v251\n\t"
    "v_lshlrev_b32 v249, 16, v130\n\t"
    "v_and_b32 v251, 0xffff0000, v130\n\t"
    "v_mul_f32 v249, v249, v250\n\t"
    "v_mul_f32 v251, v251, v250\n\t"
    "v_cvt_pk_bf16_f32 v130, v249, v251\n\t"
    "v_lshlrev_b32 v249, 16, v131\n\t"
    "v_and_b32 v251, 0xffff0000, v131\n\t"
    "v_mul_f32 v249, v249, v250\n\t"
    "v_mul_f32 v251, v251, v250\n\t"
    "v_cvt_pk_bf16_f32 v131, v249, v251\n\t"
    "v_lshlrev_b32 v249, 16, v144\n\t"
    "v_and_b32 v251, 0xffff0000, v144\n\t"
    "v_mul_f32 v249, v249, v250\n\t"
    "v_mul_f32 v251, v251, v250\n\t"
    "v_cvt_pk_bf16_f32 v144, v249, v251\n\t"
    "v_lshlrev_b32 v249, 16, v145\n\t"
    "v_and_b32 v251, 0xffff0000, v145\n\t"
    "v_mul_f32 v249, v249, v250\n\t"
    "v_mul_f32 v251, v251, v250\n\t"
    "v_cvt_pk_bf16_f32 v145, v249, v251\n\t"
    "v_lshlrev_b32 v249, 16, v146\n\t"
    "v_and_b32 v251, 0xffff0000, v146\n\t"
    "v_mul_f32 v249, v249, v250\n\t"
    "v_mul_f32 v251, v251, v250\n\t"
    "v_cvt_pk_bf16_f32 v146, v249, v251\n\t"
    "v_lshlrev_b32 v249, 16, v147\n\t"
    "v_and_b32 v251, 0xffff0000, v147\n\t"
    "v_mul_f32 v249, v249, v250\n\t"
    "v_mul_f32 v251, v251, v250\n\t"
    "v_cvt_pk_bf16_f32 v147, v249, v251\n\t"
    "v_lshlrev_b32 v249, 16, v160\n\t"
    "v_and_b32 v251, 0xffff0000, v160\n\t"
    "v_mul_f32 v249, v249, v250\n\t"
    "v_mul_f32 v251, v251, v250\n\t"
    "v_cvt_pk_bf16_f32 v160, v249, v251\n\t"
    "v_lshlrev_b32 v249, 16, v161\n\t"
    "v_and_b32 v251, 0xffff0000, v161\n\t"
    "v_mul_f32 v249, v249, v250\n\t"
    "v_mul_f32 v251, v251, v250\n\t"
    "v_cvt_pk_bf16_f32 v161, v249, v251\n\t"
    "v_lshlrev_b32 v249, 16, v162\n\t"
    "v_and_b32 v251, 0xffff0000, v162\n\t"
    "v_mul_f32 v249, v249, v250\n\t"
    "v_mul_f32 v251, v251, v250\n\t"
    "v_cvt_pk_bf16_f32 v162, v249, v251\n\t"
    "v_lshlrev_b32 v249, 16, v163\n\t"
    "v_and_b32 v251, 0xffff0000, v163\n\t"
    "v_mul_f32 v249, v249, v250\n\t"
    "v_mul_f32 v251, v251, v250\n\t"
    "v_cvt_pk_bf16_f32 v163, v249, v251\n\t"
    "v_lshlrev_b32 v249, 16, v176\n\t"
    "v_and_b32 v251, 0xffff0000, v176\n\t"
    "v_mul_f32 v249, v249, v250\n\t"
    "v_mul_f32 v251, v251, v250\n\t"
    "v_cvt_pk_bf16_f32 v176, v249, v251\n\t"
    "v_lshlrev_b32 v249, 16, v177\n\t"
    "v_and_b32 v251, 0xffff0000, v177\n\t"
    "v_mul_f32 v249, v249, v250\n\t"
    "v_mul_f32 v251, v251, v250\n\t"
    "v_cvt_pk_bf16_f32 v177, v249, v251\n\t"
    "v_lshlrev_b32 v249, 16, v178\n\t"
    "v_and_b32 v251, 0xffff0000, v178\n\t"
    "v_mul_f32 v249, v249, v250\n\t"
    "v_mul_f32 v251, v251, v250\n\t"
    "v_cvt_pk_bf16_f32 v178, v249, v251\n\t"
    "v_lshlrev_b32 v249, 16, v179\n\t"
    "v_and_b32 v251, 0xffff0000, v179\n\t"
    "v_mul_f32 v249, v249, v250\n\t"
    "v_mul_f32 v251, v251, v250\n\t"
    "v_cvt_pk_bf16_f32 v179, v249, v251\n\t"
    "v_accvgpr_read_b32 v251, a192\n\t"
    "v_mul_f32 v251, v251, v250\n\t"
    "v_accvgpr_write_b32 a192, v251\n\t"
    "v_accvgpr_read_b32 v251, a193\n\t"
    "v_mul_f32 v251, v251, v250\n\t"
    "v_accvgpr_write_b32 a193, v251\n\t"
    "v_accvgpr_read_b32 v251, a194\n\t"
    "v_mul_f32 v251, v251, v250\n\t"
    "v_accvgpr_write_b32 a194, v251\n\t"
    "v_accvgpr_read_b32 v251, a195\n\t"
    "v_mul_f32 v251, v251, v250\n\t"
    "v_accvgpr_write_b32 a195, v251\n\t"
    "s_nop 1\n\t"
    "s_setpc_b64 s[90:91]\n\t"
    ".Lrare1_%=:\n\t"
    "s_nop 7\n\t"
    "v_mov_b32 v250, v248\n\t"
    "s_nop 1\n\t"
    "v_permlane32_swap_b32 v248, v250\n\t"
    "s_nop 1\n\t"
    "v_max_f32 v248, v248, v250\n\t"
    "v_mov_b32 v250, v248\n\t"
    "s_nop 1\n\t"
    "v_permlane16_swap_b32 v248, v250\n\t"
    "s_nop 1\n\t"
    "v_max_f32 v248, v248, v250\n\t"
    "v_max_f32 v249, 0, v248\n\t"
    "v_mul_f32 v249, s53, v249\n\t"
    "v_ceil_f32 v249, v249\n\t"
    "v_exp_f32 v250, -v249\n\t"
    "v_mul_f32 v249, 0x3f7af233, v249\n\t"
    "v_sub_f32 v212, v212, v249\n\t"
    "v_sub_f32 v213, v213, v249\n\t"
    "v_sub_f32 v214, v214, v249\n\t"
    "v_sub_f32 v215, v215, v249\n\t"
    "v_sub_f32 v200, v200, v249\n\t"
    "v_sub_f32 v201, v201, v249\n\t"
    "v_sub_f32 v202, v202, v249\n\t"
    "v_sub_f32 v203, v203, v249\n\t"
    "v_sub_f32 v204, v204, v249\n\t"
    "v_sub_f32 v205, v205, v249\n\t"
    "v_sub_f32 v206, v206, v249\n\t"
    "v_sub_f32 v207, v207, v249\n\t"
    "v_mul_f32 v32, v32, v250\n\t"
    "v_mul_f32 v33, v33, v250\n\t"
    "v_mul_f32 v34, v34, v250\n\t"
    "v_mul_f32 v35, v35, v250\n\t"
    "v_mul_f32 v36, v36, v250\n\t"
    "v_mul_f32 v37, v37, v250\n\t"
    "v_mul_f32 v38, v38, v250\n\t"
    "v_mul_f32 v39, v39, v250\n\t"
    "v_mul_f32 v40, v40, v250\n\t"
    "v_mul_f32 v41, v41, v250\n\t"
    "v_mul_f32 v42, v42, v250\n\t"
    "v_mul_f32 v43, v43, v250\n\t"
    "v_mul_f32 v44, v44, v250\n\t"
    "v_mul_f32 v45, v45, v250\n\t"
    "v_mul_f32 v46, v46, v250\n\t"
    "v_mul_f32 v47, v47, v250\n\t"
    "v_mul_f32 v48, v48, v250\n\t"
    "v_mul_f32 v49, v49, v250\n\t"
    "v_mul_f32 v50, v50, v250\n\t"
    "v_mul_f32 v51, v51, v250\n\t"
    "v_mul_f32 v52, v52, v250\n\t"
    "v_mul_f32 v53, v53, v250\n\t"
    "v_mul_f32 v54, v54, v250\n\t"
    "v_mul_f32 v55, v55, v250\n\t"
    "v_mul_f32 v56, v56, v250\n\t"
    "v_mul_f32 v57, v57, v250\n\t"
    "v_mul_f32 v58, v58, v250\n\t"
    "v_mul_f32 v59, v59, v250\n\t"
    "v_mul_f32 v60, v60, v250\n\t"
    "v_mul_f32 v61, v61, v250\n\t"
    "v_mul_f32 v62, v62, v250\n\t"
    "v_mul_f32 v63, v63, v250\n\t"
    "v_lshlrev_b32 v249, 16, v132\n\t"
    "v_and_b32 v251, 0xffff0000, v132\n\t"
    "v_mul_f32 v249, v249, v250\n\t"
    "v_mul_f32 v251, v251, v250\n\t"
    "v_cvt_pk_bf16_f32 v132, v249, v251\n\t"
    "v_lshlrev_b32 v249, 16, v133\n\t"
    "v_and_b32 v251, 0xffff0000, v133\n\t"
    "v_mul_f32 v249, v249, v250\n\t"
    "v_mul_f32 v251, v251, v250\n\t"
    "v_cvt_pk_bf16_f32 v133, v249, v251\n\t"
    "v_lshlrev_b32 v249, 16, v134\n\t"
    "v_and_b32 v251, 0xffff0000, v134\n\t"
    "v_mul_f32 v249, v249, v250\n\t"
    "v_mul_f32 v251, v251, v250\n\t"
    "v_cvt_pk_bf16_f32 v134, v249, v251\n\t"
    "v_lshlrev_b32 v249, 16, v135\n\t"
    "v_and_b32 v251, 0xffff0000, v135\n\t"
    "v_mul_f32 v249, v249, v250\n\t"
    "v_mul_f32 v251, v251, v250\n\t"
    "v_cvt_pk_bf16_f32 v135, v249, v251\n\t"
    "v_lshlrev_b32 v249, 16, v148\n\t"
    "v_and_b32 v251, 0xffff0000, v148\n\t"
    "v_mul_f32 v249, v249, v250\n\t"
    "v_mul_f32 v251, v251, v250\n\t"
    "v_cvt_pk_bf16_f32 v148, v249, v251\n\t"
    "v_lshlrev_b32 v249, 16, v149\n\t"
    "v_and_b32 v251, 0xffff0000, v149\n\t"
    "v_mul_f32 v249, v249, v250\n\t"
    "v_mul_f32 v251, v251, v250\n\t"
    "v_cvt_pk_bf16_f32 v149, v249, v251\n\t"
    "v_lshlrev_b32 v249, 16, v150\n\t"
    "v_and_b32 v251, 0xffff0000, v150\n\t"
    "v_mul_f32 v249, v249, v250\n\t"
    "v_mul_f32 v251, v251, v250\n\t"
    "v_cvt_pk_bf16_f32 v150, v249, v251\n\t"
    "v_lshlrev_b32 v249, 16, v151\n\t"
    "v_and_b32 v251, 0xffff0000, v151\n\t"
    "v_mul_f32 v249, v249, v250\n\t"
    "v_mul_f32 v251, v251, v250\n\t"
    "v_cvt_pk_bf16_f32 v151, v249, v251\n\t"
    "v_lshlrev_b32 v249, 16, v164\n\t"
    "v_and_b32 v251, 0xffff0000, v164\n\t"
    "v_mul_f32 v249, v249, v250\n\t"
    "v_mul_f32 v251, v251, v250\n\t"
    "v_cvt_pk_bf16_f32 v164, v249, v251\n\t"
    "v_lshlrev_b32 v249, 16, v165\n\t"
    "v_and_b32 v251, 0xffff0000, v165\n\t"
    "v_mul_f32 v249, v249, v250\n\t"
    "v_mul_f32 v251, v251, v250\n\t"
    "v_cvt_pk_bf16_f32 v165, v249, v251\n\t"
    "v_lshlrev_b32 v249, 16, v166\n\t"
    "v_and_b32 v251, 0xffff0000, v166\n\t"
    "v_mul_f32 v249, v249, v250\n\t"
    "v_mul_f32 v251, v251, v250\n\t"
    "v_cvt_pk_bf16_f32 v166, v249, v251\n\t"
    "v_lshlrev_b32 v249, 16, v167\n\t"
    "v_and_b32 v251, 0xffff0000, v167\n\t"
    "v_mul_f32 v249, v249, v250\n\t"
    "v_mul_f32 v251, v251, v250\n\t"
    "v_cvt_pk_bf16_f32 v167, v249, v251\n\t"
    "v_lshlrev_b32 v249, 16, v180\n\t"
    "v_and_b32 v251, 0xffff0000, v180\n\t"
    "v_mul_f32 v249, v249, v250\n\t"
    "v_mul_f32 v251, v251, v250\n\t"
    "v_cvt_pk_bf16_f32 v180, v249, v251\n\t"
    "v_lshlrev_b32 v249, 16, v181\n\t"
    "v_and_b32 v251, 0xffff0000, v181\n\t"
    "v_mul_f32 v249, v249, v250\n\t"
    "v_mul_f32 v251, v251, v250\n\t"
    "v_cvt_pk_bf16_f32 v181, v249, v251\n\t"
    "v_lshlrev_b32 v249, 16, v182\n\t"
    "v_and_b32 v251, 0xffff0000, v182\n\t"
    "v_mul_f32 v249, v249, v250\n\t"
    "v_mul_f32 v251, v251, v250\n\t"
    "v_cvt_pk_bf16_f32 v182, v249, v251\n\t"
    "v_lshlrev_b32 v249, 16, v183\n\t"
    "v_and_b32 v251, 0xffff0000, v183\n\t"
    "v_mul_f32 v249, v249, v250\n\t"
    "v_mul_f32 v251, v251, v250\n\t"
    "v_cvt_pk_bf16_f32 v183, v249, v251\n\t"
    "v_accvgpr_read_b32 v251, a196\n\t"
    "v_mul_f32 v251, v251, v250\n\t"
    "v_accvgpr_write_b32 a196, v251\n\t"
    "v_accvgpr_read_b32 v251, a197\n\t"
    "v_mul_f32 v251, v251, v250\n\t"
    "v_accvgpr_write_b32 a197, v251\n\t"
    "v_accvgpr_read_b32 v251, a198\n\t"
    "v_mul_f32 v251, v251, v250\n\t"
    "v_accvgpr_write_b32 a198, v251\n\t"
    "v_accvgpr_read_b32 v251, a199\n\t"
    "v_mul_f32 v251, v251, v250\n\t"
    "v_accvgpr_write_b32 a199, v251\n\t"
    "s_nop 1\n\t"
    "s_setpc_b64 s[90:91]\n\t"
    ".Lrare2_%=:\n\t"
    "s_nop 7\n\t"
    "v_mov_b32 v250, v248\n\t"
    "s_nop 1\n\t"
    "v_permlane32_swap_b32 v248, v250\n\t"
    "s_nop 1\n\t"
    "v_max_f32 v248, v248, v250\n\t"
    "v_mov_b32 v250, v248\n\t"
    "s_nop 1\n\t"
    "v_permlane16_swap_b32 v248, v250\n\t"
    "s_nop 1\n\t"
    "v_max_f32 v248, v248, v250\n\t"
    "v_max_f32 v249, 0, v248\n\t"
    "v_mul_f32 v249, s53, v249\n\t"
    "v_ceil_f32 v249, v249\n\t"
    "v_exp_f32 v250, -v249\n\t"
    "v_mul_f32 v249, 0x3f7af233, v249\n\t"
    "v_sub_f32 v216, v216, v249\n\t"
    "v_sub_f32 v217, v217, v249\n\t"
    "v_sub_f32 v218, v218, v249\n\t"
    "v_sub_f32 v219, v219, v249\n\t"
    "v_sub_f32 v192, v192, v249\n\t"
    "v_sub_f32 v193, v193, v249\n\t"
    "v_sub_f32 v194, v194, v249\n\t"
    "v_sub_f32 v195, v195, v249\n\t"
    "v_sub_f32 v196, v196, v249\n\t"
    "v_sub_f32 v197, v197, v249\n\t"
    "v_sub_f32 v198, v198, v249\n\t"
    "v_sub_f32 v199, v199, v249\n\t"
    "v_mul_f32 v64, v64, v250\n\t"
    "v_mul_f32 v65, v65, v250\n\t"
    "v_mul_f32 v66, v66, v250\n\t"
    "v_mul_f32 v67, v67, v250\n\t"
    "v_mul_f32 v68, v68, v250\n\t"
    "v_mul_f32 v69, v69, v250\n\t"
    "v_mul_f32 v70, v70, v250\n\t"
    "v_mul_f32 v71, v71, v250\n\t"
    "v_mul_f32 v72, v72, v250\n\t"
    "v_mul_f32 v73, v73, v250\n\t"
    "v_mul_f32 v74, v74, v250\n\t"
    "v_mul_f32 v75, v75, v250\n\t"
    "v_mul_f32 v76, v76, v250\n\t"
    "v_mul_f32 v77, v77, v250\n\t"
    "v_mul_f32 v78, v78, v250\n\t"
    "v_mul_f32 v79, v79, v250\n\t"
    "v_mul_f32 v80, v80, v250\n\t"
    "v_mul_f32 v81, v81, v250\n\t"
    "v_mul_f32 v82, v82, v250\n\t"
    "v_mul_f32 v83, v83, v250\n\t"
    "v_mul_f32 v84, v84, v250\n\t"
    "v_mul_f32 v85, v85, v250\n\t"
    "v_mul_f32 v86, v86, v250\n\t"
    "v_mul_f32 v87, v87, v250\n\t"
    "v_mul_f32 v88, v88, v250\n\t"
    "v_mul_f32 v89, v89, v250\n\t"
    "v_mul_f32 v90, v90, v250\n\t"
    "v_mul_f32 v91, v91, v250\n\t"
    "v_mul_f32 v92, v92, v250\n\t"
    "v_mul_f32 v93, v93, v250\n\t"
    "v_mul_f32 v94, v94, v250\n\t"
    "v_mul_f32 v95, v95, v250\n\t"
    "v_lshlrev_b32 v249, 16, v136\n\t"
    "v_and_b32 v251, 0xffff0000, v136\n\t"
    "v_mul_f32 v249, v249, v250\n\t"
    "v_mul_f32 v251, v251, v250\n\t"
    "v_cvt_pk_bf16_f32 v136, v249, v251\n\t"
    "v_lshlrev_b32 v249, 16, v137\n\t"
    "v_and_b32 v251, 0xffff0000, v137\n\t"
    "v_mul_f32 v249, v249, v250\n\t"
    "v_mul_f32 v251, v251, v250\n\t"
    "v_cvt_pk_bf16_f32 v137, v249, v251\n\t"
    "v_lshlrev_b32 v249, 16, v138\n\t"
    "v_and_b32 v251, 0xffff0000, v138\n\t"
    "v_mul_f32 v249, v249, v250\n\t"
    "v_mul_f32 v251, v251, v250\n\t"
    "v_cvt_pk_bf16_f32 v138, v249, v251\n\t"
    "v_lshlrev_b32 v249, 16, v139\n\t"
    "v_and_b32 v251, 0xffff0000, v139\n\t"
    "v_mul_f32 v249, v249, v250\n\t"
    "v_mul_f32 v251, v251, v250\n\t"
    "v_cvt_pk_bf16_f32 v139, v249, v251\n\t"
    "v_lshlrev_b32 v249, 16, v152\n\t"
    "v_and_b32 v251, 0xffff0000, v152\n\t"
    "v_mul_f32 v249, v249, v250\n\t"
    "v_mul_f32 v251, v251, v250\n\t"
    "v_cvt_pk_bf16_f32 v152, v249, v251\n\t"
    "v_lshlrev_b32 v249, 16, v153\n\t"
    "v_and_b32 v251, 0xffff0000, v153\n\t"
    "v_mul_f32 v249, v249, v250\n\t"
    "v_mul_f32 v251, v251, v250\n\t"
    "v_cvt_pk_bf16_f32 v153, v249, v251\n\t"
    "v_lshlrev_b32 v249, 16, v154\n\t"
    "v_and_b32 v251, 0xffff0000, v154\n\t"
    "v_mul_f32 v249, v249, v250\n\t"
    "v_mul_f32 v251, v251, v250\n\t"
    "v_cvt_pk_bf16_f32 v154, v249, v251\n\t"
    "v_lshlrev_b32 v249, 16, v155\n\t"
    "v_and_b32 v251, 0xffff0000, v155\n\t"
    "v_mul_f32 v249, v249, v250\n\t"
    "v_mul_f32 v251, v251, v250\n\t"
    "v_cvt_pk_bf16_f32 v155, v249, v251\n\t"
    "v_lshlrev_b32 v249, 16, v168\n\t"
    "v_and_b32 v251, 0xffff0000, v168\n\t"
    "v_mul_f32 v249, v249, v250\n\t"
    "v_mul_f32 v251, v251, v250\n\t"
    "v_cvt_pk_bf16_f32 v168, v249, v251\n\t"
    "v_lshlrev_b32 v249, 16, v169\n\t"
    "v_and_b32 v251, 0xffff0000, v169\n\t"
    "v_mul_f32 v249, v249, v250\n\t"
    "v_mul_f32 v251, v251, v250\n\t"
    "v_cvt_pk_bf16_f32 v169, v249, v251\n\t"
    "v_lshlrev_b32 v249, 16, v170\n\t"
    "v_and_b32 v251, 0xffff0000, v170\n\t"
    "v_mul_f32 v249, v249, v250\n\t"
    "v_mul_f32 v251, v251, v250\n\t"
    "v_cvt_pk_bf16_f32 v170, v249, v251\n\t"
    "v_lshlrev_b32 v249, 16, v171\n\t"
    "v_and_b32 v251, 0xffff0000, v171\n\t"
    "v_mul_f32 v249, v249, v250\n\t"
    "v_mul_f32 v251, v251, v250\n\t"
    "v_cvt_pk_bf16_f32 v171, v249, v251\n\t"
    "v_lshlrev_b32 v249, 16, v184\n\t"
    "v_and_b32 v251, 0xffff0000, v184\n\t"
    "v_mul_f32 v249, v249, v250\n\t"
    "v_mul_f32 v251, v251, v250\n\t"
    "v_cvt_pk_bf16_f32 v184, v249, v251\n\t"
    "v_lshlrev_b32 v249, 16, v185\n\t"
    "v_and_b32 v251, 0xffff0000, v185\n\t"
    "v_mul_f32 v249, v249, v250\n\t"
    "v_mul_f32 v251, v251, v250\n\t"
    "v_cvt_pk_bf16_f32 v185, v249, v251\n\t"
    "v_lshlrev_b32 v249, 16, v186\n\t"
    "v_and_b32 v251, 0xffff0000, v186\n\t"
    "v_mul_f32 v249, v249, v250\n\t"
    "v_mul_f32 v251, v251, v250\n\t"
    "v_cvt_pk_bf16_f32 v186, v249, v251\n\t"
    "v_lshlrev_b32 v249, 16, v187\n\t"
    "v_and_b32 v251, 0xffff0000, v187\n\t"
    "v_mul_f32 v249, v249, v250\n\t"
    "v_mul_f32 v251, v251, v250\n\t"
    "v_cvt_pk_bf16_f32 v187, v249, v251\n\t"
    "v_accvgpr_read_b32 v251, a200\n\t"
    "v_mul_f32 v251, v251, v250\n\t"
    "v_accvgpr_write_b32 a200, v251\n\t"
    "v_accvgpr_read_b32 v251, a201\n\t"
    "v_mul_f32 v251, v251, v250\n\t"
    "v_accvgpr_write_b32 a201, v251\n\t"
    "v_accvgpr_read_b32 v251, a202\n\t"
    "v_mul_f32 v251, v251, v250\n\t"
    "v_accvgpr_write_b32 a202, v251\n\t"
    "v_accvgpr_read_b32 v251, a203\n\t"
    "v_mul_f32 v251, v251, v250\n\t"
    "v_accvgpr_write_b32 a203, v251\n\t"
    "s_nop 1\n\t"
    "s_setpc_b64 s[90:91]\n\t"
    ".Lrare3_%=:\n\t"
    "s_nop 7\n\t"
    "v_mov_b32 v250, v248\n\t"
    "s_nop 1\n\t"
    "v_permlane32_swap_b32 v248, v250\n\t"
    "s_nop 1\n\t"
    "v_max_f32 v248, v248, v250\n\t"
    "v_mov_b32 v250, v248\n\t"
    "s_nop 1\n\t"
    "v_permlane16_swap_b32 v248, v250\n\t"
    "s_nop 1\n\t"
    "v_max_f32 v248, v248, v250\n\t"
    "v_max_f32 v249, 0, v248\n\t"
    "v_mul_f32 v249, s53, v249\n\t"
    "v_ceil_f32 v249, v249\n\t"
    "v_exp_f32 v250, -v249\n\t"
    "v_mul_f32 v249, 0x3f7af233, v249\n\t"
    "v_sub_f32 v220, v220, v249\n\t"
    "v_sub_f32 v221, v221, v249\n\t"
    "v_sub_f32 v222, v222, v249\n\t"
    "v_sub_f32 v223, v223, v249\n\t"
    "v_sub_f32 v200, v200, v249\n\t"
    "v_sub_f32 v201, v201, v249\n\t"
    "v_sub_f32 v202, v202, v249\n\t"
    "v_sub_f32 v203, v203, v249\n\t"
    "v_sub_f32 v204, v204, v249\n\t"
    "v_sub_f32 v205, v205, v249\n\t"
    "v_sub_f32 v206, v206, v249\n\t"
    "v_sub_f32 v207, v207, v249\n\t"
    "v_mul_f32 v96, v96, v250\n\t"
    "v_mul_f32 v97, v97, v250\n\t"
    "v_mul_f32 v98, v98, v250\n\t"
    "v_mul_f32 v99, v99, v250\n\t"
    "v_mul_f32 v100, v100, v250\n\t"
    "v_mul_f32 v101, v101, v250\n\t"
    "v_mul_f32 v102, v102, v250\n\t"
    "v_mul_f32 v103, v103, v250\n\t"
    "v_mul_f32 v104, v104, v250\n\t"
    "v_mul_f32 v105, v105, v250\n\t"
    "v_mul_f32 v106, v106, v250\n\t"
    "v_mul_f32 v107, v107, v250\n\t"
    "v_mul_f32 v108, v108, v250\n\t"
    "v_mul_f32 v109, v109, v250\n\t"
    "v_mul_f32 v110, v110, v250\n\t"
    "v_mul_f32 v111, v111, v250\n\t"
    "v_mul_f32 v112, v112, v250\n\t"
    "v_mul_f32 v113, v113, v250\n\t"
    "v_mul_f32 v114, v114, v250\n\t"
    "v_mul_f32 v115, v115, v250\n\t"
    "v_mul_f32 v116, v116, v250\n\t"
    "v_mul_f32 v117, v117, v250\n\t"
    "v_mul_f32 v118, v118, v250\n\t"
    "v_mul_f32 v119, v119, v250\n\t"
    "v_mul_f32 v120, v120, v250\n\t"
    "v_mul_f32 v121, v121, v250\n\t"
    "v_mul_f32 v122, v122, v250\n\t"
    "v_mul_f32 v123, v123, v250\n\t"
    "v_mul_f32 v124, v124, v250\n\t"
    "v_mul_f32 v125, v125, v250\n\t"
    "v_mul_f32 v126, v126, v250\n\t"
    "v_mul_f32 v127, v127, v250\n\t"
    "v_lshlrev_b32 v249, 16, v140\n\t"
    "v_and_b32 v251, 0xffff0000, v140\n\t"
    "v_mul_f32 v249, v249, v250\n\t"
    "v_mul_f32 v251, v251, v250\n\t"
    "v_cvt_pk_bf16_f32 v140, v249, v251\n\t"
    "v_lshlrev_b32 v249, 16, v141\n\t"
    "v_and_b32 v251, 0xffff0000, v141\n\t"
    "v_mul_f32 v249, v249, v250\n\t"
    "v_mul_f32 v251, v251, v250\n\t"
    "v_cvt_pk_bf16_f32 v141, v249, v251\n\t"
    "v_lshlrev_b32 v249, 16, v142\n\t"
    "v_and_b32 v251, 0xffff0000, v142\n\t"
    "v_mul_f32 v249, v249, v250\n\t"
    "v_mul_f32 v251, v251, v250\n\t"
    "v_cvt_pk_bf16_f32 v142, v249, v251\n\t"
    "v_lshlrev_b32 v249, 16, v143\n\t"
    "v_and_b32 v251, 0xffff0000, v143\n\t"
    "v_mul_f32 v249, v249, v250\n\t"
    "v_mul_f32 v251, v251, v250\n\t"
    "v_cvt_pk_bf16_f32 v143, v249, v251\n\t"
    "v_lshlrev_b32 v249, 16, v156\n\t"
    "v_and_b32 v251, 0xffff0000, v156\n\t"
    "v_mul_f32 v249, v249, v250\n\t"
    "v_mul_f32 v251, v251, v250\n\t"
    "v_cvt_pk_bf16_f32 v156, v249, v251\n\t"
    "v_lshlrev_b32 v249, 16, v157\n\t"
    "v_and_b32 v251, 0xffff0000, v157\n\t"
    "v_mul_f32 v249, v249, v250\n\t"
    "v_mul_f32 v251, v251, v250\n\t"
    "v_cvt_pk_bf16_f32 v157, v249, v251\n\t"
    "v_lshlrev_b32 v249, 16, v158\n\t"
    "v_and_b32 v251, 0xffff0000, v158\n\t"
    "v_mul_f32 v249, v249, v250\n\t"
    "v_mul_f32 v251, v251, v250\n\t"
    "v_cvt_pk_bf16_f32 v158, v249, v251\n\t"
    "v_lshlrev_b32 v249, 16, v159\n\t"
    "v_and_b32 v251, 0xffff0000, v159\n\t"
    "v_mul_f32 v249, v249, v250\n\t"
    "v_mul_f32 v251, v251, v250\n\t"
    "v_cvt_pk_bf16_f32 v159, v249, v251\n\t"
    "v_lshlrev_b32 v249, 16, v172\n\t"
    "v_and_b32 v251, 0xffff0000, v172\n\t"
    "v_mul_f32 v249, v249, v250\n\t"
    "v_mul_f32 v251, v251, v250\n\t"
    "v_cvt_pk_bf16_f32 v172, v249, v251\n\t"
    "v_lshlrev_b32 v249, 16, v173\n\t"
    "v_and_b32 v251, 0xffff0000, v173\n\t"
    "v_mul_f32 v249, v249, v250\n\t"
    "v_mul_f32 v251, v251, v250\n\t"
    "v_cvt_pk_bf16_f32 v173, v249, v251\n\t"
    "v_lshlrev_b32 v249, 16, v174\n\t"
    "v_and_b32 v251, 0xffff0000, v174\n\t"
    "v_mul_f32 v249, v249, v250\n\t"
    "v_mul_f32 v251, v251, v250\n\t"
    "v_cvt_pk_bf16_f32 v174, v249, v251\n\t"
    "v_lshlrev_b32 v249, 16, v175\n\t"
    "v_and_b32 v251, 0xffff0000, v175\n\t"
    "v_mul_f32 v249, v249, v250\n\t"
    "v_mul_f32 v251, v251, v250\n\t"
    "v_cvt_pk_bf16_f32 v175, v249, v251\n\t"
    "v_lshlrev_b32 v249, 16, v188\n\t"
    "v_and_b32 v251, 0xffff0000, v188\n\t"
    "v_mul_f32 v249, v249, v250\n\t"
    "v_mul_f32 v251, v251, v250\n\t"
    "v_cvt_pk_bf16_f32 v188, v249, v251\n\t"
    "v_lshlrev_b32 v249, 16, v189\n\t"
    "v_and_b32 v251, 0xffff0000, v189\n\t"
    "v_mul_f32 v249, v249, v250\n\t"
    "v_mul_f32 v251, v251, v250\n\t"
    "v_cvt_pk_bf16_f32 v189, v249, v251\n\t"
    "v_lshlrev_b32 v249, 16, v190\n\t"
    "v_and_b32 v251, 0xffff0000, v190\n\t"
    "v_mul_f32 v249, v249, v250\n\t"
    "v_mul_f32 v251, v251, v250\n\t"
    "v_cvt_pk_bf16_f32 v190, v249, v251\n\t"
    "v_lshlrev_b32 v249, 16, v191\n\t"
    "v_and_b32 v251, 0xffff0000, v191\n\t"
    "v_mul_f32 v249, v249, v250\n\t"
    "v_mul_f32 v251, v251, v250\n\t"
    "v_cvt_pk_bf16_f32 v191, v249, v251\n\t"
    "v_accvgpr_read_b32 v251, a204\n\t"
    "v_mul_f32 v251, v251, v250\n\t"
    "v_accvgpr_write_b32 a204, v251\n\t"
    "v_accvgpr_read_b32 v251, a205\n\t"
    "v_mul_f32 v251, v251, v250\n\t"
    "v_accvgpr_write_b32 a205, v251\n\t"
    "v_accvgpr_read_b32 v251, a206\n\t"
    "v_mul_f32 v251, v251, v250\n\t"
    "v_accvgpr_write_b32 a206, v251\n\t"
    "v_accvgpr_read_b32 v251, a207\n\t"
    "v_mul_f32 v251, v251, v250\n\t"
    "v_accvgpr_write_b32 a207, v251\n\t"
    "s_nop 1\n\t"
    "s_setpc_b64 s[90:91]\n\t"
    : [tid] "+{v0}"(tid)
    : [ka] "s"(__builtin_amdgcn_kernarg_segment_ptr()), [blk] "s"(blockIdx.x)
    : "v1", "v2", "v3", "v4", "v5", "v6", "v7", "v8", "v9", "v10", "v11", "v12", "v13", "v14", "v15", "v16", "v17", "v18", "v19", "v20", "v21", "v22", "v23", "v24", "v25", "v26", "v27", "v28", "v29", "v30", "v31", "v32", "v33", "v34", "v35", "v36", "v37", "v38", "v39", "v40", "v41", "v42", "v43", "v44", "v45", "v46", "v47", "v48", "v49", "v50", "v51", "v52", "v53", "v54", "v55", "v56", "v57", "v58", "v59", "v60", "v61", "v62", "v63", "v64", "v65", "v66", "v67", "v68", "v69", "v70", "v71", "v72", "v73", "v74", "v75", "v76", "v77", "v78", "v79", "v80", "v81", "v82", "v83", "v84", "v85", "v86", "v87", "v88", "v89", "v90", "v91", "v92", "v93", "v94", "v95", "v96", "v97", "v98", "v99", "v100", "v101", "v102", "v103", "v104", "v105", "v106", "v107", "v108", "v109", "v110", "v111", "v112", "v113", "v114", "v115", "v116", "v117", "v118", "v119", "v120", "v121", "v122", "v123", "v124", "v125", "v126", "v127", "v128", "v129", "v130", "v131", "v132", "v133", "v134", "v135", "v136", "v137", "v138", "v139", "v140", "v141", "v142", "v143", "v144", "v145", "v146", "v147", "v148", "v149", "v150", "v151", "v152", "v153", "v154", "v155", "v156", "v157", "v158", "v159", "v160", "v161", "v162", "v163", "v164", "v165", "v166", "v167", "v168", "v169", "v170", "v171", "v172", "v173", "v174", "v175", "v176", "v177", "v178", "v179", "v180", "v181", "v182", "v183", "v184", "v185", "v186", "v187", "v188", "v189", "v190", "v191", "v192", "v193", "v194", "v195", "v196", "v197", "v198", "v199", "v200", "v201", "v202", "v203", "v204", "v205", "v206", "v207", "v208", "v209", "v210", "v211", "v212", "v213", "v214", "v215", "v216", "v217", "v218", "v219", "v220", "v221", "v222", "v223", "v224", "v225", "v226", "v227", "v228", "v229", "v230", "v231", "v232", "v233", "v234", "v235", "v236", "v237", "v238", "v239", "v240", "v241", "v242", "v243", "v244", "v245", "v246", "v247", "v248", "v249", "v250", "v251", "v252", "v253", "v254", "v255", "a0", "a1", "a2", "a3", "a4", "a5", "a6", "a7", "a8", "a9", "a10", "a11", "a12", "a13", "a14", "a15", "a16", "a17", "a18", "a19", "a20", "a21", "a22", "a23", "a24", "a25", "a26", "a27", "a28", "a29", "a30", "a31", "a32", "a33", "a34", "a35", "a36", "a37", "a38", "a39", "a40", "a41", "a42", "a43", "a44", "a45", "a46", "a47", "a48", "a49", "a50", "a51", "a52", "a53", "a54", "a55", "a56", "a57", "a58", "a59", "a60", "a61", "a62", "a63", "a64", "a65", "a66", "a67", "a68", "a69", "a70", "a71", "a72", "a73", "a74", "a75", "a76", "a77", "a78", "a79", "a80", "a81", "a82", "a83", "a84", "a85", "a86", "a87", "a88", "a89", "a90", "a91", "a92", "a93", "a94", "a95", "a96", "a97", "a98", "a99", "a100", "a101", "a102", "a103", "a104", "a105", "a106", "a107", "a108", "a109", "a110", "a111", "a112", "a113", "a114", "a115", "a116", "a117", "a118", "a119", "a120", "a121", "a122", "a123", "a124", "a125", "a126", "a127", "a128", "a129", "a130", "a131", "a132", "a133", "a134", "a135", "a136", "a137", "a138", "a139", "a140", "a141", "a142", "a143", "a144", "a145", "a146", "a147", "a148", "a149", "a150", "a151", "a152", "a153", "a154", "a155", "a156", "a157", "a158", "a159", "a160", "a161", "a162", "a163", "a164", "a165", "a166", "a167", "a168", "a169", "a170", "a171", "a172", "a173", "a174", "a175", "a176", "a177", "a178", "a179", "a180", "a181", "a182", "a183", "a184", "a185", "a186", "a187", "a188", "a189", "a190", "a191", "a192", "a193", "a194", "a195", "a196", "a197", "a198", "a199", "a200", "a201", "a202", "a203", "a204", "a205", "a206", "a207", "a208", "a209", "a210", "a211", "a212", "a213", "a214", "a215", "a216", "a217", "a218", "a219", "a220", "a221", "a222", "a223", "a224", "a225", "a226", "a227", "a228", "a229", "a230", "a231", "a232", "a233", "a234", "a235", "a236", "a237", "a238", "a239", "s8", "s9", "s10", "s11", "s12", "s13", "s14", "s15", "s16", "s17", "s18", "s19", "s20", "s21", "s22", "s23", "s24", "s25", "s26", "s27", "s28", "s29", "s30", "s31", "s33", "s34", "s35", "s36", "s37", "s38", "s39", "s40", "s41", "s42", "s43", "s44", "s45", "s46", "s47", "s48", "s49", "s50", "s51", "s52", "s53", "s54", "s55", "s56", "s57", "s58", "s59", "s60", "s61", "s62", "s63", "s64", "s65", "s66", "s67", "s68", "s69", "s70", "s71", "s72", "s73", "s74", "s75", "s76", "s77", "s78", "s79", "s80", "s81", "s82", "s83", "s84", "s85", "s86", "s87", "s88", "s89", "s90", "s91", "s92", "s93", "s94", "s95", "s96", "s97", "s98", "s99", "vcc", "scc", "memory");
}

extern "C" void kernel_launch(void* const* d_in, const int* in_sizes, int n_in, void* d_out, int out_size, void* d_ws, size_t ws_size, hipStream_t stream) {
  (void)in_sizes; (void)n_in; (void)out_size; (void)ws_size;
  (void)hipFuncSetAttribute((const void*)tree_attn, hipFuncAttributeMaxDynamicSharedMemorySize, 0x28000);
  hipLaunchKernelGGL(tree_attn, dim3(256), dim3(256), 0x28000, stream, d_in[0], d_in[1], d_in[2], d_in[3], d_in[4], d_in[5], d_ws, d_out);
}
```
